```python
import torch

def _softmax(x, axis=-1):
    return torch.softmax(x, dim=axis)
def _gelu(x):
    return torch.nn.functional.gelu(x, approximate="tanh")
def _silu(x):
    return torch.nn.functional.silu(x)
def _sigmoid(x):
    return torch.sigmoid(x)
def _layernorm(x, axis=-1, eps=1e-5):
    m = x.mean(dim=axis, keepdim=True)
    v = x.var(dim=axis, keepdim=True, unbiased=False)
    return (x - m) / torch.sqrt(v + eps)
def _scatter_add(tgt, idx, src, axis=0):
    return tgt.clone().index_add_(axis, idx, src)
def _segment_sum(src, seg, n):
    out = torch.zeros((n,) + src.shape[1:], dtype=src.dtype)
    return out.index_add_(0, seg, src)
def _pad(x, pw):
    flat = tuple(v for pair in reversed(pw) for v in pair)
    return torch.nn.functional.pad(x, flat)

kernel_name = 'gated_mix__ssm_conv_s4d__linear_attn_block__prompt'

def setup_inputs():

    _w = (lambda a: (lambda t: t.to(torch.bfloat16) if t.is_floating_point() else t)(torch.as_tensor(a)))
    def _A_setup():
        Bsz, L, D, N = 1, 2048, 640, 192
        return {
            "u": _w((lambda *s: torch.randn(*s, dtype=torch.bfloat16) * 0.05)(Bsz, L, D)),
            "log_A_real": _w((lambda *s: torch.randn(*s, dtype=torch.bfloat16) * 6.4)(D, N) * 0.5),
            "A_imag": _w((lambda *s: torch.randn(*s, dtype=torch.bfloat16) * 0.05)(D, N) * 3.14),
            "C_real": _w((lambda *s: torch.randn(*s, dtype=torch.bfloat16) * 0.2)(D, N) / N ** 0.5),
            "C_imag": _w((lambda *s: torch.randn(*s, dtype=torch.bfloat16) * 0.2)(D, N) / N ** 0.5),
            "log_dt": _w((lambda *s: torch.randn(*s, dtype=torch.bfloat16) * 0.05)(D) * 0.5 - 4.0),
            "D_skip": _w((lambda *s: torch.randn(*s, dtype=torch.bfloat16) * 0.05)(D)),
        }
    _A_in = _A_setup()

    def _B_setup():
        B, S, D = 1, 2048, 640
        return {
            "x": _w((lambda *s: torch.randn(*s, dtype=torch.bfloat16) * 0.05)(B, S, D)),
            "wqkv": _w((lambda *s: torch.randn(*s, dtype=torch.bfloat16) * 0.8)(D, 3 * D)),
            "wo": _w((lambda *s: torch.randn(*s, dtype=torch.bfloat16) * 0.05)(D, D)),
        }
    _B_in = _B_setup()

    return {
        "A_u": _A_in["u"],
        "A_log_A_real": _A_in["log_A_real"],
        "A_A_imag": _A_in["A_imag"],
        "A_C_real": _A_in["C_real"],
        "A_C_imag": _A_in["C_imag"],
        "A_log_dt": _A_in["log_dt"],
        "A_D_skip": _A_in["D_skip"],
        "B_x": _B_in["x"],
        "B_wqkv": _B_in["wqkv"],
        "B_wo": _B_in["wo"],
        "gate_w": _w((lambda *s: torch.randn(*s, dtype=torch.bfloat16) * 0.05)(640) * 16.0),
    }

def reference(A_u, A_log_A_real, A_A_imag, A_C_real, A_C_imag, A_log_dt, A_D_skip, B_x, B_wqkv, B_wo, gate_w):
    def _A_ref(u, log_A_real, A_imag, C_real, C_imag, log_dt, D_skip):
        L = u.shape[1]
        dt = torch.exp(log_dt)
        A = -torch.exp(log_A_real) + 1j * A_imag
        C = (C_real + 1j * C_imag) * dt[:, None]
        pos = (lambda x, dt: x.to(dt))(torch.arange(L), log_dt.dtype)
        P = torch.exp((A * dt[:, None])[:, :, None] * pos[None, None, :])
        K = 2.0 * torch.einsum("dn,dnl->dl", C, P).real
        L2 = 2 * L
        u_t = (lambda x, *d: x.permute(*d))(u, 0, 2, 1)
        Kf = (lambda x, *a, **k: torch.fft.rfft(x.float(), *a, **k))(K, L2)
        Uf = (lambda x, *a, **k: torch.fft.rfft(x.float(), *a, **k))(u_t, L2)
        y = (lambda x, *a, **k: torch.fft.irfft(x, *a, **k).to(torch.bfloat16))(Uf * Kf[None, :, :], L2)[:, :, :L]
        y = (lambda x, *d: x.permute(*d))(y, 0, 2, 1)
        return y + u * D_skip[None, None, :]

    def _B_ref(x, wqkv, wo):
        B, S, D = x.shape
        H = 32; Dh = D // H
        qkv = (lambda x, *d: x.permute(*d))((x @ wqkv).reshape(B, S, 3, H, Dh), 2, 0, 3, 1, 4)
        q, k, v = qkv[0], qkv[1], qkv[2]
        phi = lambda t: torch.where(t > 0, t + 1, torch.exp(t)) + 1e-6
        pq, pk = phi(q), phi(k)
        kv = torch.einsum("bhsd,bhse->bhde", pk, v)
        z = torch.einsum("bhsd->bhd", pk)
        num = torch.einsum("bhqd,bhde->bhqe", pq, kv)
        den = torch.einsum("bhqd,bhd->bhq", pq, z)[..., None] + 1e-6
        o = num / den
        return (lambda x, *d: x.permute(*d))(o, 0, 2, 1, 3).reshape(B, S, D) @ wo

    x = A_u
    g = _sigmoid((x * gate_w).sum(-1, keepdims=True))
    a = _A_ref(x, A_log_A_real, A_A_imag, A_C_real, A_C_imag, A_log_dt, A_D_skip)
    b = _B_ref(x, B_wqkv, B_wo)
    return g * a + (1 - g) * b
```

```cpp
#include <hip/hip_runtime.h>
#include <hip/hip_bf16.h>
#include <stdint.h>

typedef unsigned short u16;
__device__ __forceinline__ float bf2f(u16 v) { return __uint_as_float(((unsigned)v) << 16); }
__device__ __forceinline__ u16 f2bf(float f) { __hip_bfloat16 b = __float2bfloat16(f); return *reinterpret_cast<u16*>(&b); }
__device__ __forceinline__ float rbf(float f) { return bf2f(f2bf(f)); }

constexpr int L = 2048, D = 640, N = 192, H = 32, DH = 20;

__global__ void k_gen(const u16* logA, const u16* Aim, const u16* Cr, const u16* Ci, const u16* logdt, float* K) {
    int d = blockIdx.x;
    float dt = rbf(expf(bf2f(logdt[d])));
    for (int l = threadIdx.x; l < L; l += blockDim.x) {
        float pos = rbf((float)l);
        float acc = 0.f;
        for (int n = 0; n < N; ++n) {
            float ar = -rbf(expf(bf2f(logA[d * N + n])));
            float ai = bf2f(Aim[d * N + n]);
            float zr = ar * dt, zi = ai * dt;
            float cr = bf2f(Cr[d * N + n]) * dt, ci = bf2f(Ci[d * N + n]) * dt;
            float e = expf(zr * pos);
            float s, c;
            sincosf(zi * pos, &s, &c);
            acc += cr * e * c - ci * e * s;
        }
        K[d * L + l] = 2.f * acc;
    }
}

__global__ void conv_k(const u16* u, const float* K, const u16* Dskip, u16* a) {
    int d = blockIdx.x;
    __shared__ float su[L], sk[L];
    for (int l = threadIdx.x; l < L; l += blockDim.x) { su[l] = bf2f(u[l * D + d]); sk[l] = K[d * L + l]; }
    __syncthreads();
    float ds = bf2f(Dskip[d]);
    for (int t = threadIdx.x; t < L; t += blockDim.x) {
        float acc = 0.f;
        for (int j = 0; j <= t; ++j) acc += sk[j] * su[t - j];
        float y = rbf(acc);
        a[t * D + d] = f2bf(y + rbf(su[t] * ds));
    }
}

__device__ __forceinline__ float phi(float t) {
    float r = t > 0.f ? rbf(t + 1.f) : rbf(expf(t));
    return rbf(r + 1e-6f);
}

__global__ void qkv_k(const u16* x, const u16* w, u16* qkv) {
    int l = blockIdx.x;
    __shared__ float sx[D];
    for (int i = threadIdx.x; i < D; i += blockDim.x) sx[i] = bf2f(x[l * D + i]);
    __syncthreads();
    for (int n = threadIdx.x; n < 3 * D; n += blockDim.x) {
        float acc = 0.f;
        for (int k = 0; k < D; ++k) acc += sx[k] * bf2f(w[k * 3 * D + n]);
        float v = rbf(acc);
        if (n < 2 * D) v = phi(v);
        qkv[l * 3 * D + n] = f2bf(v);
    }
}

__global__ void kv_k(const u16* qkv, float* kv) {
    int h = blockIdx.x;
    int t = threadIdx.x;
    if (t >= 420) return;
    int dd = t / 21, e = t % 21;
    float acc = 0.f;
    for (int s = 0; s < L; ++s) {
        float pk = bf2f(qkv[s * 3 * D + D + h * DH + dd]);
        float v = e < 20 ? bf2f(qkv[s * 3 * D + 2 * D + h * DH + e]) : 1.f;
        acc += pk * v;
    }
    kv[h * 420 + t] = rbf(acc);
}

__global__ void o_k(const u16* qkv, const float* kv, u16* o) {
    int l = blockIdx.x;
    for (int c = threadIdx.x; c < D; c += blockDim.x) {
        int h = c / DH, e = c % DH;
        float num = 0.f, den = 0.f;
        for (int dd = 0; dd < DH; ++dd) {
            float pq = bf2f(qkv[l * 3 * D + h * DH + dd]);
            num += pq * kv[h * 420 + dd * 21 + e];
            den += pq * kv[h * 420 + dd * 21 + 20];
        }
        num = rbf(num);
        den = rbf(rbf(den) + 1e-6f);
        o[l * D + c] = f2bf(num / den);
    }
}

__global__ void out_k(const u16* x, const u16* o, const u16* wo, const u16* gw, const u16* a, u16* out) {
    int l = blockIdx.x;
    __shared__ float so[D];
    __shared__ float red[256];
    float part = 0.f;
    for (int i = threadIdx.x; i < D; i += blockDim.x) { so[i] = bf2f(o[l * D + i]); part += rbf(bf2f(x[l * D + i]) * bf2f(gw[i])); }
    red[threadIdx.x] = part;
    __syncthreads();
    for (int s = 128; s > 0; s >>= 1) { if (threadIdx.x < s) red[threadIdx.x] += red[threadIdx.x + s]; __syncthreads(); }
    float gs = rbf(red[0]);
    float g = rbf(1.f / (1.f + expf(-gs)));
    float omg = rbf(1.f - g);
    for (int n = threadIdx.x; n < D; n += blockDim.x) {
        float acc = 0.f;
        for (int k = 0; k < D; ++k) acc += so[k] * bf2f(wo[k * D + n]);
        float b = rbf(acc);
        out[l * D + n] = f2bf(rbf(g * bf2f(a[l * D + n])) + rbf(omg * b));
    }
}

extern "C" void kernel_launch(void* const* d_in, const int* in_sizes, int n_in,
                              void* d_out, int out_size, void* d_ws, size_t ws_size,
                              hipStream_t stream) {
    const u16* u = (const u16*)d_in[0];
    const u16* logA = (const u16*)d_in[1];
    const u16* Aim = (const u16*)d_in[2];
    const u16* Cr = (const u16*)d_in[3];
    const u16* Ci = (const u16*)d_in[4];
    const u16* logdt = (const u16*)d_in[5];
    const u16* Dskip = (const u16*)d_in[6];
    const u16* wqkv = (const u16*)d_in[8];
    const u16* wo = (const u16*)d_in[9];
    const u16* gw = (const u16*)d_in[10];
    char* ws = (char*)d_ws;
    float* K = (float*)ws; ws += (size_t)D * L * 4;
    u16* a = (u16*)ws; ws += (size_t)D * L * 2;
    u16* qkv = (u16*)ws; ws += (size_t)3 * D * L * 2;
    float* kv = (float*)ws; ws += 32 * 420 * 4;
    u16* o = (u16*)ws; ws += (size_t)D * L * 2;
    k_gen<<<D, 256, 0, stream>>>(logA, Aim, Cr, Ci, logdt, K);
    conv_k<<<D, 256, 0, stream>>>(u, K, Dskip, a);
    qkv_k<<<L, 256, 0, stream>>>(u, wqkv, qkv);
    kv_k<<<H, 448, 0, stream>>>(qkv, kv);
    o_k<<<L, 256, 0, stream>>>(qkv, kv, o);
    out_k<<<L, 256, 0, stream>>>(u, o, wo, gw, a, (u16*)d_out);
}
```

```cpp
#include <hip/hip_runtime.h>
#include <hip/hip_bf16.h>
#include <stdint.h>

typedef unsigned short u16;
typedef short bf16x8 __attribute__((ext_vector_type(8)));
typedef short s16x4 __attribute__((ext_vector_type(4)));
typedef float f32x2 __attribute__((ext_vector_type(2)));
typedef float f32x4 __attribute__((ext_vector_type(4)));
typedef float f32x16 __attribute__((ext_vector_type(16)));
typedef unsigned u32x4 __attribute__((ext_vector_type(4)));
typedef unsigned u32x2 __attribute__((ext_vector_type(2)));
typedef __bf16 bfv2 __attribute__((ext_vector_type(2)));
#define LDS __attribute__((address_space(3)))
#define DI __device__ __forceinline__

constexpr int L = 2048, D = 640, NS = 192, H = 32, DH = 20;

DI float bf2f(u16 v) { return __uint_as_float(((unsigned)v) << 16); }
DI unsigned pack2(float a, float b) {
    f32x2 f = {a, b};
    bfv2 v = __builtin_convertvector(f, bfv2);
    return __builtin_bit_cast(unsigned, v);
}
DI float lo2f(unsigned p) { return __uint_as_float(p << 16); }
DI float hi2f(unsigned p) { return __uint_as_float(p & 0xffff0000u); }
DI float rbf(float f) { return lo2f(pack2(f, 0.f)); }
DI float phi(float t) {
    float r = t > 0.f ? rbf(t + 1.f) : rbf(__expf(t));
    return rbf(r + 1e-6f);
}

constexpr size_t WS_KH = 0;
constexpr size_t WS_KL = WS_KH + (size_t)D * L * 2;
constexpr size_t WS_UT = WS_KL + (size_t)D * L * 2;
constexpr size_t WS_G = WS_UT + (size_t)D * L * 2;
constexpr size_t WS_PQ = WS_G + (size_t)L * 4;
constexpr size_t WS_KVP = WS_PQ + (size_t)D * L * 2;
constexpr size_t WS_O = WS_KVP + (size_t)16 * 32 * 448 * 4;
constexpr size_t WS_AT = WS_O + (size_t)D * L * 2;
constexpr size_t WS_END = WS_AT + (size_t)D * L * 2;

constexpr int G1_ASTG = 16384, G1_BROW = 352, G1_BSTG = 64 * G1_BROW, G1_STG = G1_ASTG + G1_BSTG;
constexpr int K1_LDS = 2 * G1_STG;

template <bool IS_Q>
DI void gemm_qkv(char* smem, const u16* __restrict__ x, const u16* __restrict__ W, int rt, int cg,
                 u16* __restrict__ pq, float* __restrict__ kvp) {
    const int tid = threadIdx.x, wave = tid >> 6, lane = tid & 63;
    const int wm = wave >> 1, wn = wave & 1;
    const int row0 = rt * 128;
    const char* a_src[4];
#pragma unroll
    for (int it = 0; it < 4; ++it) {
        int id = it * 256 + tid, row = id >> 3, pc = id & 7, lc = pc ^ (row & 7);
        a_src[it] = (const char*)x + (size_t)(row0 + row) * 1280 + lc * 16;
    }
    const char* b_src[6];
#pragma unroll
    for (int it = 0; it < 6; ++it) {
        int id = it * 256 + tid;
        if (id >= 1408) id = 0;
        int prow = id / 22, pc = id - prow * 22;
        if (pc >= 20) pc = 0;
        int blk = prow >> 5, pos = prow & 31;
        int k = blk * 32 + 8 * (2 * (pos >> 4) + ((pos >> 2) & 1)) + 4 * ((pos >> 3) & 1) + (pos & 3);
        int col = IS_Q ? cg * 160 + pc * 8 : (pc < 10 ? 640 + 80 * cg + pc * 8 : 1280 + 80 * cg + (pc - 10) * 8);
        b_src[it] = (const char*)W + (size_t)k * 3840 + col * 2;
    }
    auto stage = [&](int buf, int kt) {
        char* sa = smem + buf * G1_STG;
        char* sb = sa + G1_ASTG;
#pragma unroll
        for (int it = 0; it < 4; ++it)
            __builtin_amdgcn_global_load_lds((const unsigned*)(a_src[it] + kt * 128), (LDS unsigned*)(sa + it * 4096 + tid * 16), 16, 0, 0);
#pragma unroll
        for (int it = 0; it < 5; ++it)
            __builtin_amdgcn_global_load_lds((const unsigned*)(b_src[it] + (size_t)kt * 64 * 3840), (LDS unsigned*)(sb + it * 4096 + tid * 16), 16, 0, 0);
        if (wave < 2)
            __builtin_amdgcn_global_load_lds((const unsigned*)(b_src[5] + (size_t)kt * 64 * 3840), (LDS unsigned*)(sb + 5 * 4096 + tid * 16), 16, 0, 0);
    };
    f32x4 acc[4][5];
#pragma unroll
    for (int i = 0; i < 4; ++i)
#pragma unroll
        for (int j = 0; j < 5; ++j) acc[i][j] = (f32x4){0.f, 0.f, 0.f, 0.f};
    const int li = lane & 15, kg = lane >> 4, q = li >> 2, p = lane & 3;
    int a_off[4];
#pragma unroll
    for (int mi = 0; mi < 4; ++mi) {
        int row = wm * 64 + mi * 16 + li;
        a_off[mi] = row * 128 + ((kg ^ (row & 7)) * 16);
    }
    const int b_off = (16 * (kg >> 1) + 4 * (kg & 1) + q) * G1_BROW + wn * 160 + p * 8;

    stage(0, 0);
    for (int kt = 0; kt < 10; ++kt) {
        asm volatile("s_waitcnt vmcnt(0)" ::: "memory");
        __syncthreads();
        if (kt + 1 < 10) stage((kt + 1) & 1, kt + 1);
        char* sa = smem + (kt & 1) * G1_STG;
        char* sb = sa + G1_ASTG;
#pragma unroll
        for (int ks = 0; ks < 2; ++ks) {
            bf16x8 af[4], bfr[5];
#pragma unroll
            for (int mi = 0; mi < 4; ++mi) af[mi] = *(const bf16x8*)(sa + (a_off[mi] ^ (ks * 64)));
#pragma unroll
            for (int ni = 0; ni < 5; ++ni) {
                s16x4 t0 = __builtin_amdgcn_ds_read_tr16_b64_v4i16((LDS s16x4*)(sb + b_off + ks * 32 * G1_BROW + ni * 32));
                s16x4 t1 = __builtin_amdgcn_ds_read_tr16_b64_v4i16((LDS s16x4*)(sb + b_off + (ks * 32 + 8) * G1_BROW + ni * 32));
                bfr[ni] = __builtin_shufflevector(t0, t1, 0, 1, 2, 3, 4, 5, 6, 7);
            }
#pragma unroll
            for (int mi = 0; mi < 4; ++mi)
#pragma unroll
                for (int ni = 0; ni < 5; ++ni)
                    acc[mi][ni] = IS_Q ? __builtin_amdgcn_mfma_f32_16x16x32_bf16(bfr[ni], af[mi], acc[mi][ni], 0, 0, 0)
                                       : __builtin_amdgcn_mfma_f32_16x16x32_bf16(af[mi], bfr[ni], acc[mi][ni], 0, 0, 0);
        }
    }
    if (IS_Q) {
#pragma unroll
        for (int mi = 0; mi < 4; ++mi)
#pragma unroll
            for (int ni = 0; ni < 5; ++ni) {
                int m = row0 + wm * 64 + mi * 16 + li;
                int n = cg * 160 + wn * 80 + ni * 16 + 4 * kg;
                u32x2 v;
                v[0] = pack2(phi(rbf(acc[mi][ni][0])), phi(rbf(acc[mi][ni][1])));
                v[1] = pack2(phi(rbf(acc[mi][ni][2])), phi(rbf(acc[mi][ni][3])));
                *(u32x2*)(pq + (size_t)m * D + n) = v;
            }
    } else {
        __syncthreads();
#pragma unroll
        for (int mi = 0; mi < 4; ++mi)
#pragma unroll
            for (int ni = 0; ni < 5; ++ni) {
                int col = wn * 80 + ni * 16 + li;
                int row = wm * 64 + mi * 16 + 4 * kg;
                u32x2 v;
                if (wn == 0) {
                    v[0] = pack2(phi(rbf(acc[mi][ni][0])), phi(rbf(acc[mi][ni][1])));
                    v[1] = pack2(phi(rbf(acc[mi][ni][2])), phi(rbf(acc[mi][ni][3])));
                } else {
                    v[0] = pack2(acc[mi][ni][0], acc[mi][ni][1]);
                    v[1] = pack2(acc[mi][ni][2], acc[mi][ni][3]);
                }
                *(u32x2*)(smem + col * 272 + row * 2) = v;
            }
        __syncthreads();
        const int r = lane & 31, h = lane >> 5;
        f32x16 kacc;
#pragma unroll
        for (int i = 0; i < 16; ++i) kacc[i] = 0.f;
        const bf16x8 ones = {0x3f80, 0x3f80, 0x3f80, 0x3f80, 0x3f80, 0x3f80, 0x3f80, 0x3f80};
#pragma unroll
        for (int s = 0; s < 8; ++s) {
            bf16x8 ka = *(const bf16x8*)(smem + (20 * wave + r) * 272 + (16 * s + 8 * h) * 2);
            bf16x8 vb = *(const bf16x8*)(smem + (80 + 20 * wave + r) * 272 + (16 * s + 8 * h) * 2);
            if (r == 20) vb = ones;
            kacc = __builtin_amdgcn_mfma_f32_32x32x16_bf16(ka, vb, kacc, 0, 0, 0);
        }
        float* dst = kvp + ((size_t)rt * 32 + cg * 4 + wave) * 448;
        if (r <= 20) {
#pragma unroll
            for (int reg = 0; reg < 16; ++reg) {
                int i = (reg & 3) + 8 * (reg >> 2) + 4 * h;
                if (i < 20) dst[i * 21 + r] = kacc[reg];
            }
        }
    }
}

DI void kgen_channel(char* smem, int d, const u16* __restrict__ logA, const u16* __restrict__ Aim,
                     const u16* __restrict__ Cr, const u16* __restrict__ Ci, const u16* __restrict__ logdt,
                     u16* __restrict__ KH, u16* __restrict__ KL) {
    const int tid = threadIdx.x, wave = tid >> 6, lane = tid & 63;
    f32x4* params = (f32x4*)smem;
    float* red = (float*)(smem + 4096);
    if (tid < NS) {
        float dt = rbf(expf(bf2f(logdt[d])));
        float ar = -rbf(expf(bf2f(logA[d * NS + tid])));
        float ai = bf2f(Aim[d * NS + tid]);
        float zr = ar * dt, zi = ai * dt;
        f32x4 pr;
        pr[0] = zr * 1.44269504088896341f;
        pr[1] = zi * 0.15915494309189535f;
        pr[2] = 2.f * (bf2f(Cr[d * NS + tid]) * dt);
        pr[3] = 2.f * (bf2f(Ci[d * NS + tid]) * dt);
        params[tid] = pr;
    }
    __syncthreads();
    const int i = lane & 15, kk = lane >> 4;
    float Pt[3];
    Pt[0] = (float)(128 * (i >> 3) + (i & 7));
    Pt[1] = (float)(i < 8 ? 256 + 128 * (i >> 2) + 2 * (i & 3) : 512 + 128 * ((i - 8) >> 1) + 4 * (i & 1));
    Pt[2] = (float)(i < 8 ? 1024 + 128 * i : (i == 8 ? 2048 : 0));
    const float Q = 8.f * i;
    f32x4 acc[3];
#pragma unroll
    for (int t = 0; t < 3; ++t) acc[t] = (f32x4){0.f, 0.f, 0.f, 0.f};
#pragma unroll 2
    for (int s = 0; s < 12; ++s) {
        f32x4 pr = params[48 * wave + 4 * s + kk];
        float ey = __builtin_amdgcn_exp2f(pr[0] * Q);
        float ty = __builtin_amdgcn_fractf(pr[1] * Q);
        float yr = ey * __builtin_amdgcn_cosf(ty), yi = ey * __builtin_amdgcn_sinf(ty);
#pragma unroll
        for (int t = 0; t < 3; ++t) {
            float e = __builtin_amdgcn_exp2f(pr[0] * Pt[t]);
            float th = __builtin_amdgcn_fractf(pr[1] * Pt[t]);
            float vr = e * __builtin_amdgcn_cosf(th), vi = e * __builtin_amdgcn_sinf(th);
            float m0 = pr[3] * vi; asm volatile("" : "+v"(m0));
            float xr = __builtin_fmaf(pr[2], vr, -m0);
            float m1 = pr[3] * vr; asm volatile("" : "+v"(m1));
            float xin = -__builtin_fmaf(pr[2], vi, m1);
            acc[t] = __builtin_amdgcn_mfma_f32_16x16x4f32(xr, yr, acc[t], 0, 0, 0);
            acc[t] = __builtin_amdgcn_mfma_f32_16x16x4f32(xin, yi, acc[t], 0, 0, 0);
        }
    }
#pragma unroll
    for (int t = 0; t < 3; ++t)
#pragma unroll
        for (int r = 0; r < 4; ++r) red[wave * 768 + t * 256 + (4 * kk + r) * 16 + i] = acc[t][r];
    __syncthreads();
    unsigned hi[4], lo[4];
    float kv[8];
#pragma unroll
    for (int e = 0; e < 8; ++e) {
        int l = 8 * tid + e;
        int pp = (int)rbf((float)l);
        int idx;
        if (pp < 256) idx = (8 * (pp >> 7) + (pp & 7)) * 16 + ((pp & 127) >> 3);
        else if (pp < 512) { int qq = pp - 256; idx = 256 + (4 * (qq >> 7) + ((qq & 7) >> 1)) * 16 + ((qq & 127) >> 3); }
        else if (pp < 1024) { int qq = pp - 512; idx = 256 + (8 + 2 * (qq >> 7) + ((qq & 7) >> 2)) * 16 + ((qq & 127) >> 3); }
        else if (pp < 2048) { int qq = pp - 1024; idx = 512 + (qq >> 7) * 16 + ((qq & 127) >> 3); }
        else idx = 512 + 8 * 16;
        kv[e] = (red[idx] + red[768 + idx]) + (red[1536 + idx] + red[2304 + idx]);
    }
#pragma unroll
    for (int e = 0; e < 4; ++e) {
        hi[e] = pack2(kv[2 * e], kv[2 * e + 1]);
        lo[e] = pack2(kv[2 * e] - lo2f(hi[e]), kv[2 * e + 1] - hi2f(hi[e]));
    }
    *(u32x4*)(KH + (size_t)d * L + 8 * tid) = (u32x4){hi[0], hi[1], hi[2], hi[3]};
    *(u32x4*)(KL + (size_t)d * L + 8 * tid) = (u32x4){lo[0], lo[1], lo[2], lo[3]};
}

__global__ __launch_bounds__(256, 2) void k1(const u16* __restrict__ x, const u16* __restrict__ W,
                                             const u16* __restrict__ logA, const u16* __restrict__ Aim,
                                             const u16* __restrict__ Cr, const u16* __restrict__ Ci,
                                             const u16* __restrict__ logdt, const u16* __restrict__ gw,
                                             char* __restrict__ ws) {
    extern __shared__ __attribute__((aligned(16))) char smem[];
    const int bid = blockIdx.x;
    if (bid < 128) {
        gemm_qkv<false>(smem, x, W, bid >> 3, bid & 7, (u16*)(ws + WS_PQ), (float*)(ws + WS_KVP));
    } else if (bid < 192) {
        int b = bid - 128;
        gemm_qkv<true>(smem, x, W, b >> 2, b & 3, (u16*)(ws + WS_PQ), (float*)(ws + WS_KVP));
    } else {
        const int j = bid - 192;
        const int tid = threadIdx.x, wave = tid >> 6, lane = tid & 63;
        {
            const int r0 = 64 * (j / 10), c0 = 64 * (j % 10);
            char* tl = smem + 20480;
#pragma unroll
            for (int it = 0; it < 2; ++it) {
                int id = it * 256 + tid, row = id >> 3, ch = id & 7;
                u32x4 v = *(const u32x4*)(x + (size_t)(r0 + row) * D + c0 + ch * 8);
                *(u32x4*)(tl + row * 144 + ch * 16) = v;
            }
            __syncthreads();
            u16* uT = (u16*)(ws + WS_UT);
#pragma unroll
            for (int it = 0; it < 2; ++it) {
                int id = it * 256 + tid, col = id >> 3, ch = id & 7;
                unsigned o[4];
#pragma unroll
                for (int e = 0; e < 4; ++e) {
                    unsigned a = *(const u16*)(tl + (ch * 8 + 2 * e) * 144 + col * 2);
                    unsigned b = *(const u16*)(tl + (ch * 8 + 2 * e + 1) * 144 + col * 2);
                    o[e] = a | (b << 16);
                }
                *(u32x4*)(uT + (size_t)(c0 + col) * L + r0 + ch * 8) = (u32x4){o[0], o[1], o[2], o[3]};
            }
        }
        if (j < 256) {
            float* g = (float*)(ws + WS_G);
#pragma unroll
            for (int rr = 0; rr < 2; ++rr) {
                int row = 8 * j + 2 * wave + rr;
                float part = 0.f;
#pragma unroll
                for (int it = 0; it < 5; ++it) {
                    unsigned xv = *(const unsigned*)(x + (size_t)row * D + 2 * (lane + 64 * it));
                    unsigned wv = *(const unsigned*)(gw + 2 * (lane + 64 * it));
                    unsigned pr = pack2(lo2f(xv) * lo2f(wv), hi2f(xv) * hi2f(wv));
                    part += lo2f(pr) + hi2f(pr);
                }
#pragma unroll
                for (int o = 32; o > 0; o >>= 1) part += __shfl_xor(part, o);
                if (lane == 0) {
                    float gs = rbf(part);
                    g[row] = rbf(1.f / (1.f + expf(-gs)));
                }
            }
        }
        kgen_channel(smem, 2 * j, logA, Aim, Cr, Ci, logdt, (u16*)(ws + WS_KH), (u16*)(ws + WS_KL));
        __syncthreads();
        kgen_channel(smem, 2 * j + 1, logA, Aim, Cr, Ci, logdt, (u16*)(ws + WS_KH), (u16*)(ws + WS_KL));
    }
}

constexpr int C_UC = 2104 * 16;
constexpr int C_KH = C_UC, C_KL = C_UC + 6144;
constexpr int K2_LDS = C_UC + 2 * 6144;

template <bool CT0>
DI void conv_delta(const char* smem, int delta, int toff, int koff0, const bf16x8& Tm2, const bf16x8& Tm1,
                   bf16x8& To2, bf16x8& To3, f32x4 (&acc)[4][2], int li, int kg) {
    const char* tp = smem + toff + delta * 64 * 16;
    bf16x8 T0 = *(const bf16x8*)(tp);
    bf16x8 T1 = *(const bf16x8*)(tp + 16 * 16);
    bf16x8 T2 = *(const bf16x8*)(tp + 32 * 16);
    bf16x8 T3 = *(const bf16x8*)(tp + 48 * 16);
#pragma unroll
    for (int ct = CT0 ? 0 : 1; ct < 2; ++ct) {
        int bb = 16 * ct + li - delta + 16;
        int base = bb * 128;
        int x0 = ((kg) ^ (bb & 7)) * 16;
        int x1 = ((4 + kg) ^ (bb & 7)) * 16;
        bf16x8 kh1 = *(const bf16x8*)(smem + C_KH + base + x1);
        bf16x8 kl1 = *(const bf16x8*)(smem + C_KL + base + x1);
        bf16x8 kh0 = *(const bf16x8*)(smem + C_KH + base + x0);
        bf16x8 kl0 = *(const bf16x8*)(smem + C_KL + base + x0);
        acc[0][ct] = __builtin_amdgcn_mfma_f32_16x16x32_bf16(Tm2, kh1, acc[0][ct], 0, 0, 0);
        acc[1][ct] = __builtin_amdgcn_mfma_f32_16x16x32_bf16(Tm1, kh1, acc[1][ct], 0, 0, 0);
        acc[2][ct] = __builtin_amdgcn_mfma_f32_16x16x32_bf16(T0, kh1, acc[2][ct], 0, 0, 0);
        acc[3][ct] = __builtin_amdgcn_mfma_f32_16x16x32_bf16(T1, kh1, acc[3][ct], 0, 0, 0);
        acc[0][ct] = __builtin_amdgcn_mfma_f32_16x16x32_bf16(Tm2, kl1, acc[0][ct], 0, 0, 0);
        acc[1][ct] = __builtin_amdgcn_mfma_f32_16x16x32_bf16(Tm1, kl1, acc[1][ct], 0, 0, 0);
        acc[2][ct] = __builtin_amdgcn_mfma_f32_16x16x32_bf16(T0, kl1, acc[2][ct], 0, 0, 0);
        acc[3][ct] = __builtin_amdgcn_mfma_f32_16x16x32_bf16(T1, kl1, acc[3][ct], 0, 0, 0);
        acc[0][ct] = __builtin_amdgcn_mfma_f32_16x16x32_bf16(T0, kh0, acc[0][ct], 0, 0, 0);
        acc[1][ct] = __builtin_amdgcn_mfma_f32_16x16x32_bf16(T1, kh0, acc[1][ct], 0, 0, 0);
        acc[2][ct] = __builtin_amdgcn_mfma_f32_16x16x32_bf16(T2, kh0, acc[2][ct], 0, 0, 0);
        acc[3][ct] = __builtin_amdgcn_mfma_f32_16x16x32_bf16(T3, kh0, acc[3][ct], 0, 0, 0);
        acc[0][ct] = __builtin_amdgcn_mfma_f32_16x16x32_bf16(T0, kl0, acc[0][ct], 0, 0, 0);
        acc[1][ct] = __builtin_amdgcn_mfma_f32_16x16x32_bf16(T1, kl0, acc[1][ct], 0, 0, 0);
        acc[2][ct] = __builtin_amdgcn_mfma_f32_16x16x32_bf16(T2, kl0, acc[2][ct], 0, 0, 0);
        acc[3][ct] = __builtin_amdgcn_mfma_f32_16x16x32_bf16(T3, kl0, acc[3][ct], 0, 0, 0);
    }
    To2 = T2;
    To3 = T3;
}

DI void conv_role(char* smem, int d, const u16* __restrict__ uT, const u16* __restrict__ KH,
                  const u16* __restrict__ KL, const u16* __restrict__ Dskip, u16* __restrict__ aT) {
    const int tid = threadIdx.x, wave = tid >> 6, lane = tid & 63;
    u32x4 cur = *(const u32x4*)(uT + (size_t)d * L + 8 * tid);
    u32x4 prev = {0u, 0u, 0u, 0u};
    if (tid > 0) prev = *(const u32x4*)(uT + (size_t)d * L + 8 * tid - 8);
    u32x4 kh = *(const u32x4*)(KH + (size_t)d * L + 8 * tid);
    u32x4 kl = *(const u32x4*)(KL + (size_t)d * L + 8 * tid);
    const float ds = bf2f(Dskip[d]);
    {
        const u32x4 z = {0u, 0u, 0u, 0u};
        if (tid < 56) *(u32x4*)(smem + tid * 16) = z;
        *(u32x4*)(smem + C_KH + (tid >> 7) * 6144 + (tid & 127) * 16) = z;
        int bb = (tid >> 3) + 16, c = tid & 7;
        int sc = (c & 4) | ((c & 1) << 1) | ((c >> 1) & 1);
        int off = bb * 128 + ((sc ^ (bb & 7)) * 16);
        *(u32x4*)(smem + C_KH + off) = kh;
        *(u32x4*)(smem + C_KL + off) = kl;
        unsigned c8[8] = {prev[0], prev[1], prev[2], prev[3], cur[0], cur[1], cur[2], cur[3]};
        unsigned w[14];
#pragma unroll
        for (int xx = -6; xx <= 7; ++xx) {
            int m = (xx + 8) >> 1;
            if ((xx & 1) != 0) w[xx + 6] = __builtin_amdgcn_perm(c8[m], c8[m], 0x01000302u);
            else w[xx + 6] = __builtin_amdgcn_perm(c8[m - 1], c8[m], 0x07060100u);
        }
#pragma unroll
        for (int e = 0; e < 8; ++e) {
            u32x4 ch = {w[e + 6], w[e + 4], w[e + 2], w[e]};
            *(u32x4*)(smem + (8 * tid + e + 56) * 16) = ch;
        }
    }
    __syncthreads();
    const int li = lane & 15, kg = lane >> 4;
    const int kp = ((kg & 1) << 4) | ((kg >> 1) << 3);
    const int toff = (li - kp + 56) * 16;
    f32x4 acc[4][2];
#pragma unroll
    for (int i = 0; i < 4; ++i) { acc[i][0] = (f32x4){0.f, 0.f, 0.f, 0.f}; acc[i][1] = (f32x4){0.f, 0.f, 0.f, 0.f}; }
    int d0, d1, d2;
    if (wave == 0) { d0 = 0; d1 = 6; d2 = 6; }
    else if (wave == 1) { d0 = 6; d1 = 12; d2 = 12; }
    else if (wave == 2) { d0 = 12; d1 = 16; d2 = 20; }
    else { d0 = 20; d1 = 20; d2 = 32; }
    bf16x8 Ta = *(const bf16x8*)(smem + toff + (d0 * 64 - 32) * 16);
    bf16x8 Tb = *(const bf16x8*)(smem + toff + (d0 * 64 - 16) * 16);
    for (int dl = d0; dl < d1; ++dl) conv_delta<true>(smem, dl, toff, 0, Ta, Tb, Ta, Tb, acc, li, kg);
    for (int dl = d1; dl < d2; ++dl) conv_delta<false>(smem, dl, toff, 0, Ta, Tb, Ta, Tb, acc, li, kg);
    __syncthreads();
    float* red = (float*)smem;
#pragma unroll
    for (int rt = 0; rt < 4; ++rt)
#pragma unroll
        for (int ct = 0; ct < 2; ++ct) {
            if (ct == 0 && wave == 3) continue;
            int a = 16 * ct + li;
            *(f32x4*)(red + wave * 2176 + a * 68 + 16 * rt + 4 * kg) = acc[rt][ct];
        }
    __syncthreads();
    {
        int t = 8 * tid;
        int ro = t + 4 * (t >> 6);
        f32x4 s0 = *(const f32x4*)(red + ro), s1 = *(const f32x4*)(red + ro + 4);
        s0 += *(const f32x4*)(red + 2176 + ro); s1 += *(const f32x4*)(red + 2176 + ro + 4);
        s0 += *(const f32x4*)(red + 2 * 2176 + ro); s1 += *(const f32x4*)(red + 2 * 2176 + ro + 4);
        if (tid >= 128) { s0 += *(const f32x4*)(red + 3 * 2176 + ro); s1 += *(const f32x4*)(red + 3 * 2176 + ro + 4); }
        float y[8] = {s0[0], s0[1], s0[2], s0[3], s1[0], s1[1], s1[2], s1[3]};
        unsigned o[4];
#pragma unroll
        for (int e = 0; e < 4; ++e) {
            unsigned yb = pack2(y[2 * e], y[2 * e + 1]);
            unsigned sk = pack2(lo2f(cur[e]) * ds, hi2f(cur[e]) * ds);
            o[e] = pack2(lo2f(yb) + lo2f(sk), hi2f(yb) + hi2f(sk));
        }
        *(u32x4*)(aT + (size_t)d * L + t) = (u32x4){o[0], o[1], o[2], o[3]};
    }
}

DI void o_role(char* smem, int rt, int hg, const u16* __restrict__ pq, const float* __restrict__ kvp,
               u16* __restrict__ o) {
    const int tid = threadIdx.x, wave = tid >> 6, lane = tid & 63;
    u16* kvT = (u16*)smem;
    for (int i = tid; i < 4 * 1024 / 8; i += 256) *(u32x4*)(kvT + i * 8) = (u32x4){0u, 0u, 0u, 0u};
    __syncthreads();
    for (int i = tid; i < 4 * 420; i += 256) {
        int hh = i / 420, r = i - hh * 420, dd = r / 21, e = r - dd * 21;
        float s = 0.f;
#pragma unroll
        for (int pp = 0; pp < 16; ++pp) s += kvp[((size_t)pp * 32 + hg * 4 + hh) * 448 + r];
        kvT[hh * 1024 + e * 32 + dd] = (u16)(pack2(s, 0.f) & 0xffffu);
    }
    __syncthreads();
    const int li = lane & 15, kg = lane >> 4;
    const int hd = hg * 4 + wave;
    bf16x8 b0 = *(const bf16x8*)(kvT + wave * 1024 + li * 32 + kg * 8);
    bf16x8 b1 = *(const bf16x8*)(kvT + wave * 1024 + (16 + li) * 32 + kg * 8);
#pragma unroll 2
    for (int mi = 0; mi < 8; ++mi) {
        int m = rt * 128 + mi * 16 + li;
        const u16* src = pq + (size_t)m * D + hd * DH + kg * 8;
        u32x2 p0 = {0u, 0u}, p1 = {0u, 0u};
        if (kg < 3) p0 = *(const u32x2*)(src);
        if (kg < 2) p1 = *(const u32x2*)(src + 4);
        u32x4 pv = {p0[0], p0[1], p1[0], p1[1]};
        bf16x8 a = __builtin_bit_cast(bf16x8, pv);
        f32x4 z = {0.f, 0.f, 0.f, 0.f};
        f32x4 n0 = __builtin_amdgcn_mfma_f32_16x16x32_bf16(b0, a, z, 0, 0, 0);
        f32x4 n1 = __builtin_amdgcn_mfma_f32_16x16x32_bf16(b1, a, z, 0, 0, 0);
        float den = __shfl(n1[0], li + 16);
        den = rbf(rbf(den) + 1e-6f);
        u32x2 v;
        v[0] = pack2(rbf(n0[0]) / den, rbf(n0[1]) / den);
        v[1] = pack2(rbf(n0[2]) / den, rbf(n0[3]) / den);
        *(u32x2*)(o + (size_t)m * D + hd * DH + 4 * kg) = v;
        if (kg == 0) {
            v[0] = pack2(rbf(n1[0]) / den, rbf(n1[1]) / den);
            v[1] = pack2(rbf(n1[2]) / den, rbf(n1[3]) / den);
            *(u32x2*)(o + (size_t)m * D + hd * DH + 16) = v;
        }
    }
}

__global__ __launch_bounds__(256, 3) void k2(const u16* __restrict__ Dskip, char* __restrict__ ws) {
    extern __shared__ __attribute__((aligned(16))) char smem[];
    const int bid = blockIdx.x;
    if (bid < 640) {
        conv_role(smem, bid, (const u16*)(ws + WS_UT), (const u16*)(ws + WS_KH), (const u16*)(ws + WS_KL), Dskip,
                  (u16*)(ws + WS_AT));
    } else {
        int b = bid - 640;
        o_role(smem, b >> 3, b & 7, (const u16*)(ws + WS_PQ), (const float*)(ws + WS_KVP), (u16*)(ws + WS_O));
    }
}

constexpr int G3_ASTG = 8192, G3_BSTG = 64 * 160, G3_STG = G3_ASTG + G3_BSTG;
constexpr int K3_LDS = 2 * G3_STG;

__global__ __launch_bounds__(256, 2) void k3(const u16* __restrict__ wo, const char* __restrict__ ws,
                                             u16* __restrict__ out) {
    extern __shared__ __attribute__((aligned(16))) char smem[];
    const u16* o = (const u16*)(ws + WS_O);
    const u16* aT = (const u16*)(ws + WS_AT);
    const float* g = (const float*)(ws + WS_G);
    const int tid = threadIdx.x, wave = tid >> 6, lane = tid & 63;
    const int rt = blockIdx.x >> 3, cg = blockIdx.x & 7;
    const int row0 = rt * 64, col0 = cg * 80;
    const char* a_src[2];
#pragma unroll
    for (int it = 0; it < 2; ++it) {
        int id = it * 256 + tid, row = id >> 3, pc = id & 7, lc = pc ^ (row & 7);
        a_src[it] = (const char*)o + (size_t)(row0 + row) * 1280 + lc * 16;
    }
    const char* b_src[3];
#pragma unroll
    for (int it = 0; it < 3; ++it) {
        int id = it * 256 + tid;
        if (id >= 640) id = 0;
        int prow = id / 10, pc = id - prow * 10;
        int blk = prow >> 5, pos = prow & 31;
        int k = blk * 32 + 8 * (2 * (pos >> 4) + ((pos >> 2) & 1)) + 4 * ((pos >> 3) & 1) + (pos & 3);
        b_src[it] = (const char*)wo + (size_t)k * 1280 + (col0 + pc * 8) * 2;
    }
    auto stage = [&](int buf, int kt) {
        char* sa = smem + buf * G3_STG;
        char* sb = sa + G3_ASTG;
#pragma unroll
        for (int it = 0; it < 2; ++it)
            __builtin_amdgcn_global_load_lds((const unsigned*)(a_src[it] + kt * 128), (LDS unsigned*)(sa + it * 4096 + tid * 16), 16, 0, 0);
#pragma unroll
        for (int it = 0; it < 2; ++it)
            __builtin_amdgcn_global_load_lds((const unsigned*)(b_src[it] + (size_t)kt * 64 * 1280), (LDS unsigned*)(sb + it * 4096 + tid * 16), 16, 0, 0);
        if (wave < 2)
            __builtin_amdgcn_global_load_lds((const unsigned*)(b_src[2] + (size_t)kt * 64 * 1280), (LDS unsigned*)(sb + 2 * 4096 + tid * 16), 16, 0, 0);
    };
    f32x4 acc[5];
#pragma unroll
    for (int j = 0; j < 5; ++j) acc[j] = (f32x4){0.f, 0.f, 0.f, 0.f};
    const int li = lane & 15, kg = lane >> 4, q = li >> 2, p = lane & 3;
    const int arow = wave * 16 + li;
    const int a_off = arow * 128 + ((kg ^ (arow & 7)) * 16);
    const int b_off = (16 * (kg >> 1) + 4 * (kg & 1) + q) * 160 + p * 8;
    stage(0, 0);
    for (int kt = 0; kt < 10; ++kt) {
        asm volatile("s_waitcnt vmcnt(0)" ::: "memory");
        __syncthreads();
        if (kt + 1 < 10) stage((kt + 1) & 1, kt + 1);
        char* sa = smem + (kt & 1) * G3_STG;
        char* sb = sa + G3_ASTG;
#pragma unroll
        for (int ks = 0; ks < 2; ++ks) {
            bf16x8 af = *(const bf16x8*)(sa + (a_off ^ (ks * 64)));
#pragma unroll
            for (int ni = 0; ni < 5; ++ni) {
                s16x4 t0 = __builtin_amdgcn_ds_read_tr16_b64_v4i16((LDS s16x4*)(sb + b_off + ks * 32 * 160 + ni * 32));
                s16x4 t1 = __builtin_amdgcn_ds_read_tr16_b64_v4i16((LDS s16x4*)(sb + b_off + (ks * 32 + 8) * 160 + ni * 32));
                bf16x8 bfr = __builtin_shufflevector(t0, t1, 0, 1, 2, 3, 4, 5, 6, 7);
                acc[ni] = __builtin_amdgcn_mfma_f32_16x16x32_bf16(af, bfr, acc[ni], 0, 0, 0);
            }
        }
    }
    const int m0 = row0 + wave * 16 + 4 * kg;
    f32x4 gv = *(const f32x4*)(g + m0);
#pragma unroll
    for (int ni = 0; ni < 5; ++ni) {
        int n = col0 + ni * 16 + li;
        u32x2 av = *(const u32x2*)(aT + (size_t)n * L + m0);
        float a4[4] = {lo2f(av[0]), hi2f(av[0]), lo2f(av[1]), hi2f(av[1])};
#pragma unroll
        for (int r = 0; r < 4; ++r) {
            float gg = gv[r];
            float omg = rbf(1.f - gg);
            float b = rbf(acc[ni][r]);
            float res = rbf(gg * a4[r]) + rbf(omg * b);
            out[(size_t)(m0 + r) * D + n] = (u16)(pack2(res, 0.f) & 0xffffu);
        }
    }
}

extern "C" void kernel_launch(void* const* d_in, const int* in_sizes, int n_in, void* d_out, int out_size,
                              void* d_ws, size_t ws_size, hipStream_t stream) {
    const u16* u = (const u16*)d_in[0];
    const u16* logA = (const u16*)d_in[1];
    const u16* Aim = (const u16*)d_in[2];
    const u16* Cr = (const u16*)d_in[3];
    const u16* Ci = (const u16*)d_in[4];
    const u16* logdt = (const u16*)d_in[5];
    const u16* Dskip = (const u16*)d_in[6];
    const u16* wqkv = (const u16*)d_in[8];
    const u16* wo = (const u16*)d_in[9];
    const u16* gw = (const u16*)d_in[10];
    char* ws = (char*)d_ws;
    hipFuncSetAttribute((const void*)k1, hipFuncAttributeMaxDynamicSharedMemorySize, K1_LDS);
    k1<<<512, 256, K1_LDS, stream>>>(u, wqkv, logA, Aim, Cr, Ci, logdt, gw, ws);
    k2<<<768, 256, K2_LDS, stream>>>(Dskip, ws);
    k3<<<256, 256, K3_LDS, stream>>>(wo, ws, (u16*)d_out);
}
```

```cpp
#include <hip/hip_runtime.h>
#include <hip/hip_bf16.h>
#include <stdint.h>

typedef unsigned short u16;
typedef short bf16x8 __attribute__((ext_vector_type(8)));
typedef _Float16 f16x8 __attribute__((ext_vector_type(8)));
typedef _Float16 f16x2 __attribute__((ext_vector_type(2)));
typedef short s16x4 __attribute__((ext_vector_type(4)));
typedef float f32x2 __attribute__((ext_vector_type(2)));
typedef float f32x4 __attribute__((ext_vector_type(4)));
typedef float f32x16 __attribute__((ext_vector_type(16)));
typedef unsigned u32x4 __attribute__((ext_vector_type(4)));
typedef unsigned u32x2 __attribute__((ext_vector_type(2)));
typedef __bf16 bfv2 __attribute__((ext_vector_type(2)));
#define LDS __attribute__((address_space(3)))
#define DI __device__ __forceinline__

constexpr int L = 2048, D = 640, NS = 192, H = 32, DH = 20;
#define FS(i)
#define FS_DECL
#define FS_OUT(ws, slot)
#define STAMP_BEGIN
#define STAMP_END(ws, kid)

DI float bf2f(u16 v) { return __uint_as_float(((unsigned)v) << 16); }
DI unsigned pack2(float a, float b) {
    f32x2 f = {a, b};
    bfv2 v = __builtin_convertvector(f, bfv2);
    return __builtin_bit_cast(unsigned, v);
}
DI float lo2f(unsigned p) { return __uint_as_float(p << 16); }
DI float hi2f(unsigned p) { return __uint_as_float(p & 0xffff0000u); }
DI float rbf(float f) { return lo2f(pack2(f, 0.f)); }
DI float phi(float t) {
    float r = t > 0.f ? rbf(t + 1.f) : rbf(__expf(t));
    return rbf(r + 1e-6f);
}

constexpr size_t WS_KP = 0;
constexpr size_t WS_UT = WS_KP + (size_t)D * 3 * 768 * 4;
constexpr size_t WS_G = WS_UT + (size_t)D * L * 2;
constexpr size_t WS_PQ = WS_G + (size_t)L * 4;
constexpr size_t WS_KVP = WS_PQ + (size_t)D * L * 2;
constexpr size_t WS_O = WS_KVP + (size_t)16 * 32 * 512 * 4;
constexpr size_t WS_AT = WS_O + (size_t)D * L * 2;
constexpr size_t WS_END = WS_AT + (size_t)D * L * 2;

constexpr int G1_A = 16384, G1_BOFF = 3 * G1_A, G1_BS = 10240;
constexpr int K1_LDS = G1_BOFF + 3 * G1_BS;

template <bool IS_Q>
DI void gemm_qkv(char* smem, const u16* __restrict__ x, const u16* __restrict__ W, int rt, int cg,
                 u16* __restrict__ pq, float* __restrict__ kvp) {
    constexpr int NI = IS_Q ? 4 : 5;
    constexpr int CPR = IS_Q ? 16 : 20;
    constexpr int BROW = CPR * 16;
    const int tid = threadIdx.x, lane = tid & 63;
    const int wave = __builtin_amdgcn_readfirstlane(tid >> 6);
    const int wm = wave >> 1, wn = wave & 1;
    const int row0 = rt * 128;
    FS_DECL
    FS(0)
    const char* a_src[4];
#pragma unroll
    for (int it = 0; it < 4; ++it) {
        int id = it * 256 + tid, row = id >> 3, pc = id & 7, lc = pc ^ (row & 7);
        a_src[it] = (const char*)x + (size_t)(row0 + row) * 1280 + lc * 16;
    }
    const char* b_src[3];
#pragma unroll
    for (int it = 0; it < 3; ++it) {
        int id = it * 256 + tid;
        if (id >= 32 * CPR) id = 0;
        int pos = id / CPR, pc = id - pos * CPR;
        pc ^= 2 * (IS_Q ? (pos & 7) : ((pos >> 2) & 1));
        int k = 8 * (2 * (pos >> 4) + ((pos >> 2) & 1)) + 4 * ((pos >> 3) & 1) + (pos & 3);
        int col = IS_Q ? cg * 128 + pc * 8 : (pc < 10 ? 640 + 80 * cg + pc * 8 : 1280 + 80 * cg + (pc - 10) * 8);
        b_src[it] = (const char*)W + (size_t)k * 3840 + col * 2;
    }
    auto stageA = [&](int k, int s) {
        char* sa = smem + (k % 3) * G1_A;
#pragma unroll
        for (int it = 2 * s; it < 2 * s + 2; ++it)
            __builtin_amdgcn_global_load_lds((const unsigned*)(a_src[it] + k * 128), (LDS unsigned*)(sa + it * 4096 + tid * 16), 16, 0, 0);
    };
    auto stageB = [&](int h) {
        char* sb = smem + G1_BOFF + (h % 3) * G1_BS;
#pragma unroll
        for (int it = 0; it < 2; ++it)
            __builtin_amdgcn_global_load_lds((const unsigned*)(b_src[it] + (size_t)h * 32 * 3840), (LDS unsigned*)(sb + it * 4096 + tid * 16), 16, 0, 0);
        if (!IS_Q && wave < 2)
            __builtin_amdgcn_global_load_lds((const unsigned*)(b_src[2] + (size_t)h * 32 * 3840), (LDS unsigned*)(sb + 2 * 4096 + tid * 16), 16, 0, 0);
    };
    f32x4 acc[4][NI];
#pragma unroll
    for (int i = 0; i < 4; ++i)
#pragma unroll
        for (int j = 0; j < NI; ++j) acc[i][j] = (f32x4){0.f, 0.f, 0.f, 0.f};
    const int li = lane & 15, kg = lane >> 4, q = li >> 2, p = lane & 3;
    const int a_off = (wm * 64 + li) * 128 + ((kg ^ (li & 7)) * 16);
    int b_off[NI];
    {
        const int pos0 = 16 * (kg >> 1) + 4 * (kg & 1) + q;
        const int sw = IS_Q ? (pos0 & 7) : (kg & 1);
#pragma unroll
        for (int ni = 0; ni < NI; ++ni) b_off[ni] = G1_BOFF + pos0 * BROW + (((NI * wn + ni) ^ sw) * 32) + p * 8;
    }
    stageA(0, 0); stageA(0, 1);
    stageB(0); stageA(1, 0);
    stageB(1); stageA(1, 1);
    FS(1)
#pragma unroll
    for (int h = 0; h < 20; ++h) {
        const int k = h >> 1, s = h & 1;
        constexpr int dummy = 0; (void)dummy;
        const int ap2 = (h - 2 < 16) ? 2 : 0;
        const int a1 = (h - 1 < 16) ? 2 : 0;
        const int b1 = (h - 1 + 2 < 20) ? 1 : 0;
        const int nlo = ap2 + a1 + 2 * b1;
        const int nhi = nlo + b1;
        if (!IS_Q && wave < 2) {
            switch (nhi) { case 7: asm volatile("s_waitcnt vmcnt(7)" ::: "memory"); break; case 5: asm volatile("s_waitcnt vmcnt(5)" ::: "memory"); break;
                           case 3: asm volatile("s_waitcnt vmcnt(3)" ::: "memory"); break; default: asm volatile("s_waitcnt vmcnt(0)" ::: "memory"); break; }
        } else {
            switch (nlo) { case 6: asm volatile("s_waitcnt vmcnt(6)" ::: "memory"); break; case 4: asm volatile("s_waitcnt vmcnt(4)" ::: "memory"); break;
                           case 2: asm volatile("s_waitcnt vmcnt(2)" ::: "memory"); break; default: asm volatile("s_waitcnt vmcnt(0)" ::: "memory"); break; }
        }
        asm volatile("s_waitcnt lgkmcnt(0)\n\ts_barrier" ::: "memory");
        if (h == 0) { FS(2) }
        if (h == 10) { FS(3) }
        if (h + 2 < 20) stageB(h + 2);
        if (k + 2 < 10) stageA(k + 2, s);
        const char* sa = smem + (k % 3) * G1_A;
        const char* sb = smem + (h % 3) * G1_BS;
        bf16x8 af[4], bfr[NI];
#pragma unroll
        for (int mi = 0; mi < 4; ++mi) af[mi] = *(const bf16x8*)(sa + ((a_off + mi * 2048) ^ (s * 64)));
#pragma unroll
        for (int ni = 0; ni < NI; ++ni) {
            s16x4 t0 = __builtin_amdgcn_ds_read_tr16_b64_v4i16((LDS s16x4*)(sb + b_off[ni]));
            s16x4 t1 = __builtin_amdgcn_ds_read_tr16_b64_v4i16((LDS s16x4*)(sb + b_off[ni] + 8 * BROW));
            bfr[ni] = __builtin_shufflevector(t0, t1, 0, 1, 2, 3, 4, 5, 6, 7);
        }
#pragma unroll
        for (int mi = 0; mi < 4; ++mi)
#pragma unroll
            for (int ni = 0; ni < NI; ++ni)
                acc[mi][ni] = IS_Q ? __builtin_amdgcn_mfma_f32_16x16x32_bf16(bfr[ni], af[mi], acc[mi][ni], 0, 0, 0)
                                   : __builtin_amdgcn_mfma_f32_16x16x32_bf16(af[mi], bfr[ni], acc[mi][ni], 0, 0, 0);
    }
    FS(4)
    if (IS_Q) {
#pragma unroll
        for (int mi = 0; mi < 4; ++mi)
#pragma unroll
            for (int ni = 0; ni < NI; ++ni) {
                int m = row0 + wm * 64 + mi * 16 + li;
                int n = cg * 128 + wn * 64 + ni * 16 + 4 * kg;
                u32x2 v;
                v[0] = pack2(phi(rbf(acc[mi][ni][0])), phi(rbf(acc[mi][ni][1])));
                v[1] = pack2(phi(rbf(acc[mi][ni][2])), phi(rbf(acc[mi][ni][3])));
                *(u32x2*)(pq + (size_t)m * D + n) = v;
            }
    } else {
        __syncthreads();
#pragma unroll
        for (int mi = 0; mi < 4; ++mi)
#pragma unroll
            for (int ni = 0; ni < NI; ++ni) {
                int col = wn * 80 + ni * 16 + li;
                int row = wm * 64 + mi * 16 + 4 * kg;
                u32x2 v;
                if (wn == 0) {
                    v[0] = pack2(phi(rbf(acc[mi][ni][0])), phi(rbf(acc[mi][ni][1])));
                    v[1] = pack2(phi(rbf(acc[mi][ni][2])), phi(rbf(acc[mi][ni][3])));
                } else {
                    v[0] = pack2(acc[mi][ni][0], acc[mi][ni][1]);
                    v[1] = pack2(acc[mi][ni][2], acc[mi][ni][3]);
                }
                *(u32x2*)(smem + col * 272 + row * 2) = v;
            }
        __syncthreads();
        const int r = lane & 31, h = lane >> 5;
        f32x16 kacc;
#pragma unroll
        for (int i = 0; i < 16; ++i) kacc[i] = 0.f;
        const bf16x8 ones = {0x3f80, 0x3f80, 0x3f80, 0x3f80, 0x3f80, 0x3f80, 0x3f80, 0x3f80};
#pragma unroll
        for (int s = 0; s < 8; ++s) {
            bf16x8 ka = *(const bf16x8*)(smem + (20 * wave + r) * 272 + (16 * s + 8 * h) * 2);
            bf16x8 vb = *(const bf16x8*)(smem + (80 + 20 * wave + r) * 272 + (16 * s + 8 * h) * 2);
            if (r == 20) vb = ones;
            kacc = __builtin_amdgcn_mfma_f32_32x32x16_bf16(ka, vb, kacc, 0, 0, 0);
        }
        float* dst = kvp + ((size_t)rt * 32 + cg * 4 + wave) * 512;
        if (r <= 20) {
#pragma unroll
            for (int reg = 0; reg < 16; ++reg) {
                int i = (reg & 3) + 8 * (reg >> 2) + 4 * h;
                if (i < 20) dst[i * 21 + r] = kacc[reg];
            }
        }
    }
    FS(5)
    FS_OUT((char*)pq - WS_PQ, 4)
}

struct cf { float r, i; };
DI cf cmul(cf a, cf b) {
    float t0 = a.i * b.i; asm("" : "+v"(t0));
    float t1 = a.i * b.r; asm("" : "+v"(t1));
    cf o;
    o.r = __builtin_fmaf(a.r, b.r, -t0); asm("" : "+v"(o.r));
    o.i = __builtin_fmaf(a.r, b.i, t1); asm("" : "+v"(o.i));
    return o;
}
DI cf cexpP(float a2, float f, float P) {
    float m = __builtin_amdgcn_exp2f(a2 * P);
    float th = __builtin_amdgcn_fractf(f * P);
    cf o;
    o.r = m * __builtin_amdgcn_cosf(th); asm("" : "+v"(o.r));
    o.i = m * __builtin_amdgcn_sinf(th); asm("" : "+v"(o.i));
    return o;
}
DI void put16(char* p, float a, float b) {
    f32x2 v = {a, b};
    unsigned h = __builtin_bit_cast(unsigned, __builtin_convertvector(v, f16x2));
    unsigned l;
    asm("v_fma_mixlo_f16 %0, %1, -1.0, %2 op_sel_hi:[1,0,0]" : "=v"(l) : "v"(h), "v"(a));
    asm("v_fma_mixhi_f16 %0, %1, -1.0, %2 op_sel:[1,0,0] op_sel_hi:[1,0,0]" : "+v"(l) : "v"(h), "v"(b));
    *(u32x2*)p = (u32x2){h, l};
}
constexpr int KG_P = 544, KG_S = 16 * KG_P;

DI void kgen_task(char* scr, u16 ldt, u16 la, u16 aim, u16 crr, u16 cir, float* __restrict__ kp) {
    const int lane = threadIdx.x & 63, li = lane & 15, kg = lane >> 4;
    float dt = rbf(expf(bf2f(ldt)));
    float ar = -rbf(expf(bf2f(la)));
    const float a2 = (ar * dt) * 1.44269504088896341f;
    const float f = (bf2f(aim) * dt) * 0.15915494309189535f;
    cf C = {2.f * (bf2f(crr) * dt), 2.f * (bf2f(cir) * dt)};
    float m = fmaxf(fabsf(C.r), fabsf(C.i));
#pragma unroll
    for (int o = 32; o > 0; o >>= 1) m = fmaxf(m, __shfl_xor(m, o));
    int e = (int)(__float_as_uint(m) >> 23);
    e = e < 1 ? 1 : (e > 253 ? 253 : e);
    const float sc = __uint_as_float((unsigned)(254 - e) << 23), isc = __uint_as_float((unsigned)e << 23);
    C.r *= sc; C.i *= sc;
    char* wp0 = scr + lane * 8;
    char* wp1 = scr + KG_S + lane * 8;
    const char* rp0 = scr + li * KG_P + kg * 16;
    const char* rp1 = rp0 + KG_S;
    {
        cf w8 = cexpP(a2, f, 8.f), G[4], Pb[4];
        G[1] = cexpP(a2, f, 32.f); G[2] = cexpP(a2, f, 64.f); G[3] = cmul(G[1], G[2]);
        Pb[0] = C; Pb[1] = cmul(C, w8); Pb[2] = cmul(Pb[1], w8); Pb[3] = cmul(Pb[2], w8);
#pragma unroll
        for (int a = 0; a < 4; ++a)
#pragma unroll
            for (int b = 0; b < 4; ++b) {
                cf y = a ? cmul(Pb[b], G[a]) : Pb[b];
                put16(wp0 + (4 * a + b) * KG_P, y.r, -y.i);
            }
    }
    cf F[8];
    F[0] = {1.f, 0.f};
    F[1] = cexpP(a2, f, 1.f);
    F[2] = cexpP(a2, f, 2.f);
    F[4] = cexpP(a2, f, 4.f);
    F[3] = cmul(F[2], F[1]); F[5] = cmul(F[4], F[1]); F[6] = cmul(F[4], F[2]); F[7] = cmul(F[4], F[3]);
    cf E[8];
    E[0] = {1.f, 0.f};
    E[1] = cexpP(a2, f, 128.f); E[2] = cexpP(a2, f, 256.f); E[4] = cexpP(a2, f, 512.f);
    E[3] = cmul(E[2], E[1]); E[5] = cmul(E[4], E[1]); E[6] = cmul(E[4], E[2]); E[7] = cmul(E[4], E[3]);
#pragma unroll
    for (int b = 0; b < 8; ++b) {
        put16(wp1 + b * KG_P, F[b].r, F[b].i);
        cf v = b ? cmul(E[1], F[b]) : E[1];
        put16(wp1 + (8 + b) * KG_P, v.r, v.i);
    }
    u32x4 yf[8];
#pragma unroll
    for (int s = 0; s < 8; ++s) yf[s] = *(const u32x4*)(rp0 + s * 64);
    f32x4 acc[3];
#pragma unroll
    for (int t = 0; t < 3; ++t) acc[t] = (f32x4){0.f, 0.f, 0.f, 0.f};
    auto tile_mma = [&](const char* rp, f32x4& ac) {
#pragma unroll
        for (int s = 0; s < 8; ++s) {
            f16x8 xa = *(const f16x8*)(rp + s * 64);
            u32x4 bh = {yf[s][0], yf[s][0], yf[s][2], yf[s][2]};
            u32x4 bl = {yf[s][1], yf[s][1], yf[s][3], yf[s][3]};
            ac = __builtin_amdgcn_mfma_f32_16x16x32_f16(__builtin_bit_cast(f16x8, bh), xa, ac, 0, 0, 0);
            ac = __builtin_amdgcn_mfma_f32_16x16x32_f16(__builtin_bit_cast(f16x8, bl), xa, ac, 0, 0, 0);
        }
    };
#pragma unroll
    for (int a = 0; a < 2; ++a)
#pragma unroll
        for (int b = 0; b < 4; ++b) {
            cf v = b ? cmul(E[2 + a], F[2 * b]) : E[2 + a];
            put16(wp0 + (4 * a + b) * KG_P, v.r, v.i);
        }
#pragma unroll
    for (int a = 0; a < 4; ++a) {
        put16(wp0 + (8 + 2 * a) * KG_P, E[4 + a].r, E[4 + a].i);
        cf v = cmul(E[4 + a], F[4]);
        put16(wp0 + (9 + 2 * a) * KG_P, v.r, v.i);
    }
    tile_mma(rp1, acc[0]);
    {
        cf w1024 = cexpP(a2, f, 1024.f), w2048 = cexpP(a2, f, 2048.f);
#pragma unroll
        for (int a = 0; a < 8; ++a) {
            cf v = a ? cmul(w1024, E[a]) : w1024;
            put16(wp1 + a * KG_P, v.r, v.i);
        }
        put16(wp1 + 8 * KG_P, w2048.r, w2048.i);
    }
    tile_mma(rp0, acc[1]);
    tile_mma(rp1, acc[2]);
#pragma unroll
    for (int t = 0; t < 3; ++t) *(f32x4*)(kp + t * 256 + li * 16 + 4 * kg) = acc[t] * isc;
}

__global__ __launch_bounds__(256, 2) void k1(const u16* __restrict__ x, const u16* __restrict__ W,
                                             const u16* __restrict__ logA, const u16* __restrict__ Aim,
                                             const u16* __restrict__ Cr, const u16* __restrict__ Ci,
                                             const u16* __restrict__ logdt, const u16* __restrict__ gw,
                                             char* __restrict__ ws) {
    extern __shared__ __attribute__((aligned(16))) char smem[];
    STAMP_BEGIN
    const int bid = blockIdx.x;
    if (bid < 128) {
        const int xcd = bid & 7, idx = bid >> 3;
        gemm_qkv<false>(smem, x, W, 4 * (xcd >> 1) + (idx & 3), 4 * (xcd & 1) + (idx >> 2), (u16*)(ws + WS_PQ), (float*)(ws + WS_KVP));
    } else if (bid < 208) {
        const int xcd = bid & 7, ti = (xcd & 1) * 10 + ((bid - 128) >> 3);
        gemm_qkv<true>(smem, x, W, 4 * (xcd >> 1) + (ti & 3), ti >> 2, (u16*)(ws + WS_PQ), (float*)(ws + WS_KVP));
    } else {
        const int tid = threadIdx.x, lane = tid & 63;
        const int wave = __builtin_amdgcn_readfirstlane(tid >> 6);
        const int wv = (bid - 208) * 4 + wave;
        FS_DECL
        FS(0)
        char* scr = smem + wave * (2 * KG_S);
        u16 raw[2][5];
#pragma unroll
        for (int c = 0; c < 2; ++c) {
            int task = 2 * wv + c, d = task / 3, n = (task - 3 * d) * 64 + lane;
            raw[c][0] = logdt[d];
            raw[c][1] = logA[d * NS + n];
            raw[c][2] = Aim[d * NS + n];
            raw[c][3] = Cr[d * NS + n];
            raw[c][4] = Ci[d * NS + n];
        }
        const int li = lane & 15, kg = lane >> 4;
        u32x4 tv[10], gwv[5];
        if (wv < 320) {
            const int r0 = 64 * (wv / 10), c0 = 64 * (wv % 10);
#pragma unroll
            for (int it = 0; it < 8; ++it)
                tv[it] = *(const u32x4*)(x + (size_t)(r0 + it * 8 + (lane >> 3)) * D + c0 + (lane & 7) * 8);
        } else if (wv < 576) {
            const int r0 = 8 * (wv - 320);
#pragma unroll
            for (int j = 0; j < 5; ++j) {
                gwv[j] = *(const u32x4*)(gw + (li + 16 * j) * 8);
#pragma unroll
                for (int rr = 0; rr < 2; ++rr) tv[rr * 5 + j] = *(const u32x4*)(x + (size_t)(r0 + rr * 4 + kg) * D + (li + 16 * j) * 8);
            }
        }
        float* kp = (float*)(ws + WS_KP);
        kgen_task(scr, raw[0][0], raw[0][1], raw[0][2], raw[0][3], raw[0][4], kp + (size_t)(2 * wv) * 768);
        FS(1)
        if (wv < 320) {
            const int r0 = 64 * (wv / 10), c0 = 64 * (wv % 10);
            u16* uT = (u16*)(ws + WS_UT);
#pragma unroll
            for (int it = 0; it < 8; ++it) *(u32x4*)(scr + (it * 8 + (lane >> 3)) * 144 + (lane & 7) * 16) = tv[it];
            const int q = li >> 2, p = lane & 3;
#pragma unroll
            for (int cb = 0; cb < 4; ++cb)
#pragma unroll
                for (int hh = 0; hh < 2; ++hh) {
                    const char* a = scr + (32 * hh + 8 * kg + q) * 144 + cb * 32 + p * 8;
                    s16x4 t0 = __builtin_amdgcn_ds_read_tr16_b64_v4i16((LDS s16x4*)(a));
                    s16x4 t1 = __builtin_amdgcn_ds_read_tr16_b64_v4i16((LDS s16x4*)(a + 4 * 144));
                    bf16x8 v = __builtin_shufflevector(t0, t1, 0, 1, 2, 3, 4, 5, 6, 7);
                    *(bf16x8*)(uT + (size_t)(c0 + 16 * cb + li) * L + r0 + 32 * hh + 8 * kg) = v;
                }
        } else if (wv < 576) {
            const int r0 = 8 * (wv - 320);
            float* g = (float*)(ws + WS_G);
#pragma unroll
            for (int rr = 0; rr < 2; ++rr) {
                float part = 0.f;
#pragma unroll
                for (int j = 0; j < 5; ++j)
#pragma unroll
                    for (int e = 0; e < 4; ++e) {
                        unsigned xv = tv[rr * 5 + j][e], wv2 = gwv[j][e];
                        unsigned pr = pack2(lo2f(xv) * lo2f(wv2), hi2f(xv) * hi2f(wv2));
                        part += lo2f(pr) + hi2f(pr);
                    }
#pragma unroll
                for (int o = 8; o > 0; o >>= 1) part += __shfl_xor(part, o);
                if (li == 0) {
                    float gs = rbf(part);
                    g[r0 + rr * 4 + kg] = rbf(1.f / (1.f + expf(-gs)));
                }
            }
        }
        FS(2)
        kgen_task(scr, raw[1][0], raw[1][1], raw[1][2], raw[1][3], raw[1][4], kp + (size_t)(2 * wv + 1) * 768);
        FS(3)
        FS_OUT(ws, 4)
    }
    STAMP_END(ws, 0)
}

constexpr int C_UC = 2104 * 16;
constexpr int C_KH = C_UC, C_KL = C_UC + 6144;
constexpr int C_TAB = C_UC + 2 * 6144;
constexpr int K2_LDS = C_TAB + 3072;

template <bool CT0>
DI void conv_delta(const char* smem, int delta, int toff, const bf16x8& Tm2, const bf16x8& Tm1,
                   bf16x8& To2, bf16x8& To3, f32x4 (&acc)[4][2], int li, int kg) {
    const char* tp = smem + toff + delta * 64 * 16;
    bf16x8 T0 = *(const bf16x8*)(tp);
    bf16x8 T1 = *(const bf16x8*)(tp + 16 * 16);
    bf16x8 T2 = *(const bf16x8*)(tp + 32 * 16);
    bf16x8 T3 = *(const bf16x8*)(tp + 48 * 16);
#pragma unroll
    for (int ct = CT0 ? 0 : 1; ct < 2; ++ct) {
        int bb = 16 * ct + li - delta + 16;
        int base = bb * 128;
        int x0 = ((kg) ^ (bb & 7)) * 16;
        int x1 = ((4 + kg) ^ (bb & 7)) * 16;
        bf16x8 kh1 = *(const bf16x8*)(smem + C_KH + base + x1);
        bf16x8 kl1 = *(const bf16x8*)(smem + C_KL + base + x1);
        bf16x8 kh0 = *(const bf16x8*)(smem + C_KH + base + x0);
        bf16x8 kl0 = *(const bf16x8*)(smem + C_KL + base + x0);
        acc[0][ct] = __builtin_amdgcn_mfma_f32_16x16x32_bf16(Tm2, kh1, acc[0][ct], 0, 0, 0);
        acc[1][ct] = __builtin_amdgcn_mfma_f32_16x16x32_bf16(Tm1, kh1, acc[1][ct], 0, 0, 0);
        acc[2][ct] = __builtin_amdgcn_mfma_f32_16x16x32_bf16(T0, kh1, acc[2][ct], 0, 0, 0);
        acc[3][ct] = __builtin_amdgcn_mfma_f32_16x16x32_bf16(T1, kh1, acc[3][ct], 0, 0, 0);
        acc[0][ct] = __builtin_amdgcn_mfma_f32_16x16x32_bf16(Tm2, kl1, acc[0][ct], 0, 0, 0);
        acc[1][ct] = __builtin_amdgcn_mfma_f32_16x16x32_bf16(Tm1, kl1, acc[1][ct], 0, 0, 0);
        acc[2][ct] = __builtin_amdgcn_mfma_f32_16x16x32_bf16(T0, kl1, acc[2][ct], 0, 0, 0);
        acc[3][ct] = __builtin_amdgcn_mfma_f32_16x16x32_bf16(T1, kl1, acc[3][ct], 0, 0, 0);
        acc[0][ct] = __builtin_amdgcn_mfma_f32_16x16x32_bf16(T0, kh0, acc[0][ct], 0, 0, 0);
        acc[1][ct] = __builtin_amdgcn_mfma_f32_16x16x32_bf16(T1, kh0, acc[1][ct], 0, 0, 0);
        acc[2][ct] = __builtin_amdgcn_mfma_f32_16x16x32_bf16(T2, kh0, acc[2][ct], 0, 0, 0);
        acc[3][ct] = __builtin_amdgcn_mfma_f32_16x16x32_bf16(T3, kh0, acc[3][ct], 0, 0, 0);
        acc[0][ct] = __builtin_amdgcn_mfma_f32_16x16x32_bf16(T0, kl0, acc[0][ct], 0, 0, 0);
        acc[1][ct] = __builtin_amdgcn_mfma_f32_16x16x32_bf16(T1, kl0, acc[1][ct], 0, 0, 0);
        acc[2][ct] = __builtin_amdgcn_mfma_f32_16x16x32_bf16(T2, kl0, acc[2][ct], 0, 0, 0);
        acc[3][ct] = __builtin_amdgcn_mfma_f32_16x16x32_bf16(T3, kl0, acc[3][ct], 0, 0, 0);
    }
    To2 = T2;
    To3 = T3;
}

DI void conv_role(char* smem, int d, const u16* __restrict__ uT, const float* __restrict__ kp,
                  const u16* __restrict__ Dskip, u16* __restrict__ aT) {
    const int tid = threadIdx.x, wave = tid >> 6, lane = tid & 63;
    FS_DECL
    FS(0)
    const float* kpd = kp + (size_t)d * 3 * 768 + 4 * (tid < 192 ? tid : 0);
    f32x4 k0 = *(const f32x4*)(kpd), k1v = *(const f32x4*)(kpd + 768), k2v = *(const f32x4*)(kpd + 1536);
    u32x4 cur = *(const u32x4*)(uT + (size_t)d * L + 8 * tid);
    u32x4 prev = *(const u32x4*)(uT + (size_t)d * L + 8 * (tid > 0 ? tid - 1 : 0));
    if (tid == 0) prev = (u32x4){0u, 0u, 0u, 0u};
    const float ds = bf2f(Dskip[d]);
    float* tab = (float*)(smem + C_TAB);
    {
        const u32x4 z = {0u, 0u, 0u, 0u};
        if (tid < 56) *(u32x4*)(smem + tid * 16) = z;
        *(u32x4*)(smem + C_KH + (tid >> 7) * 6144 + (tid & 127) * 16) = z;
        if (tid < 192) *(f32x4*)(tab + 4 * tid) = (k0 + k1v) + k2v;
    }
    __syncthreads();
    {
        float kv[8];
#pragma unroll
        for (int e = 0; e < 8; ++e) {
            float pf = rbf((float)(8 * tid + e));
            int pp = (int)pf;
            int ex = (int)(__float_as_uint(pf) >> 23) - 134;
            ex = ex < 0 ? 0 : (ex > 3 ? 3 : ex);
            int G = 8 * ex + ((((pp >> 7) << 3) | (pp & 7)) >> ex);
            kv[e] = tab[G * 16 + ((pp >> 3) & 15)];
        }
        unsigned hi[4], lo[4];
#pragma unroll
        for (int e = 0; e < 4; ++e) {
            hi[e] = pack2(kv[2 * e], kv[2 * e + 1]);
            lo[e] = pack2(kv[2 * e] - lo2f(hi[e]), kv[2 * e + 1] - hi2f(hi[e]));
        }
        int bb = (tid >> 3) + 16, c = tid & 7;
        int sc = (c & 4) | ((c & 1) << 1) | ((c >> 1) & 1);
        int off = bb * 128 + ((sc ^ (bb & 7)) * 16);
        *(u32x4*)(smem + C_KH + off) = (u32x4){hi[0], hi[1], hi[2], hi[3]};
        *(u32x4*)(smem + C_KL + off) = (u32x4){lo[0], lo[1], lo[2], lo[3]};
        unsigned c8[8] = {prev[0], prev[1], prev[2], prev[3], cur[0], cur[1], cur[2], cur[3]};
        unsigned w[14];
#pragma unroll
        for (int xx = -6; xx <= 7; ++xx) {
            int m = (xx + 8) >> 1;
            if ((xx & 1) != 0) w[xx + 6] = __builtin_amdgcn_perm(c8[m], c8[m], 0x01000302u);
            else w[xx + 6] = __builtin_amdgcn_perm(c8[m - 1], c8[m], 0x07060100u);
        }
#pragma unroll
        for (int e = 0; e < 8; ++e) {
            u32x4 ch = {w[e + 6], w[e + 4], w[e + 2], w[e]};
            *(u32x4*)(smem + (8 * tid + e + 56) * 16) = ch;
        }
    }
    FS(1)
    __syncthreads();
    FS(2)
    const int li = lane & 15, kg = lane >> 4;
    const int kp2 = ((kg & 1) << 4) | ((kg >> 1) << 3);
    const int toff = (li - kp2 + 56) * 16;
    f32x4 acc[4][2];
#pragma unroll
    for (int i = 0; i < 4; ++i) { acc[i][0] = (f32x4){0.f, 0.f, 0.f, 0.f}; acc[i][1] = (f32x4){0.f, 0.f, 0.f, 0.f}; }
    int d0, d1, d2;
    if (wave == 0) { d0 = 0; d1 = 6; d2 = 6; }
    else if (wave == 1) { d0 = 6; d1 = 12; d2 = 12; }
    else if (wave == 2) { d0 = 12; d1 = 16; d2 = 20; }
    else { d0 = 20; d1 = 20; d2 = 32; }
    bf16x8 Ta = *(const bf16x8*)(smem + toff + (d0 * 64 - 32) * 16);
    bf16x8 Tb = *(const bf16x8*)(smem + toff + (d0 * 64 - 16) * 16);
    for (int dl = d0; dl < d1; ++dl) conv_delta<true>(smem, dl, toff, Ta, Tb, Ta, Tb, acc, li, kg);
    for (int dl = d1; dl < d2; ++dl) conv_delta<false>(smem, dl, toff, Ta, Tb, Ta, Tb, acc, li, kg);
    FS(3)
    __syncthreads();
    FS(4)
    float* red = (float*)smem;
#pragma unroll
    for (int rt = 0; rt < 4; ++rt)
#pragma unroll
        for (int ct = 0; ct < 2; ++ct) {
            if (ct == 0 && wave == 3) continue;
            int a = 16 * ct + li;
            *(f32x4*)(red + wave * 2176 + a * 68 + 16 * rt + 4 * kg) = acc[rt][ct];
        }
    __syncthreads();
    {
        int t = 8 * tid;
        int ro = t + 4 * (t >> 6);
        f32x4 s0 = *(const f32x4*)(red + ro), s1 = *(const f32x4*)(red + ro + 4);
        s0 += *(const f32x4*)(red + 2176 + ro); s1 += *(const f32x4*)(red + 2176 + ro + 4);
        s0 += *(const f32x4*)(red + 2 * 2176 + ro); s1 += *(const f32x4*)(red + 2 * 2176 + ro + 4);
        if (tid >= 128) { s0 += *(const f32x4*)(red + 3 * 2176 + ro); s1 += *(const f32x4*)(red + 3 * 2176 + ro + 4); }
        float y[8] = {s0[0], s0[1], s0[2], s0[3], s1[0], s1[1], s1[2], s1[3]};
        unsigned o[4];
#pragma unroll
        for (int e = 0; e < 4; ++e) {
            unsigned yb = pack2(y[2 * e], y[2 * e + 1]);
            unsigned sk = pack2(lo2f(cur[e]) * ds, hi2f(cur[e]) * ds);
            o[e] = pack2(lo2f(yb) + lo2f(sk), hi2f(yb) + hi2f(sk));
        }
        *(u32x4*)(aT + (size_t)d * L + t) = (u32x4){o[0], o[1], o[2], o[3]};
    }
    FS(5)
    FS_OUT((char*)uT - WS_UT, 5)
}

DI void o_role(char* smem, int rt, int hp, const u16* __restrict__ pq, const float* __restrict__ kvp,
               u16* __restrict__ o) {
    const int tid = threadIdx.x, wave = tid >> 6, lane = tid & 63;
    const int li = lane & 15, kg = lane >> 4;
    const int hh = wave & 1, hd = 2 * hp + hh;
    const int mbase = rt * 256 + (wave >> 1) * 128;
    const int vp = tid & 127, th = tid >> 7;
    f32x4 part[16];
    {
        const float* kp = kvp + (size_t)(2 * hp + th) * 512 + 4 * (vp < 105 ? vp : 104);
#pragma unroll
        for (int pp = 0; pp < 16; ++pp) part[pp] = *(const f32x4*)(kp + (size_t)pp * 32 * 512);
    }
    u32x2 p0[8], p1[8];
    {
        const u16* src0 = pq + (size_t)(mbase + li) * D + hd * DH + (kg < 3 ? kg : 0) * 8;
        const u16* src1 = pq + (size_t)(mbase + li) * D + hd * DH + (kg < 2 ? kg : 0) * 8 + 4;
#pragma unroll
        for (int mi = 0; mi < 8; ++mi) {
            p0[mi] = *(const u32x2*)(src0 + mi * 16 * D);
            p1[mi] = *(const u32x2*)(src1 + mi * 16 * D);
        }
    }
    u16* kvT = (u16*)smem;
    *(u32x4*)(kvT + tid * 8) = (u32x4){0u, 0u, 0u, 0u};
    __syncthreads();
    if (vp < 105) {
        f32x4 s = part[0];
#pragma unroll
        for (int pp = 1; pp < 16; ++pp) s += part[pp];
#pragma unroll
        for (int c = 0; c < 4; ++c) {
            int r = 4 * vp + c, dd = r / 21, e = r - dd * 21;
            kvT[th * 1024 + e * 32 + dd] = (u16)(pack2(s[c], 0.f) & 0xffffu);
        }
    }
    __syncthreads();
    bf16x8 b0 = *(const bf16x8*)(kvT + hh * 1024 + li * 32 + kg * 8);
    bf16x8 b1 = *(const bf16x8*)(kvT + hh * 1024 + (16 + li) * 32 + kg * 8);
#pragma unroll
    for (int mi = 0; mi < 8; ++mi) {
        int m = mbase + mi * 16 + li;
        u32x4 pv = {kg < 3 ? p0[mi][0] : 0u, kg < 3 ? p0[mi][1] : 0u, kg < 2 ? p1[mi][0] : 0u, kg < 2 ? p1[mi][1] : 0u};
        bf16x8 a = __builtin_bit_cast(bf16x8, pv);
        f32x4 z = {0.f, 0.f, 0.f, 0.f};
        f32x4 n0 = __builtin_amdgcn_mfma_f32_16x16x32_bf16(b0, a, z, 0, 0, 0);
        f32x4 n1 = __builtin_amdgcn_mfma_f32_16x16x32_bf16(b1, a, z, 0, 0, 0);
        float den = __shfl(n1[0], li + 16);
        den = rbf(rbf(den) + 1e-6f);
        u32x2 v;
        v[0] = pack2(rbf(n0[0]) / den, rbf(n0[1]) / den);
        v[1] = pack2(rbf(n0[2]) / den, rbf(n0[3]) / den);
        *(u32x2*)(o + (size_t)m * D + hd * DH + 4 * kg) = v;
        if (kg == 0) {
            v[0] = pack2(rbf(n1[0]) / den, rbf(n1[1]) / den);
            v[1] = pack2(rbf(n1[2]) / den, rbf(n1[3]) / den);
            *(u32x2*)(o + (size_t)m * D + hd * DH + 16) = v;
        }
    }
}

__global__ __launch_bounds__(256, 3) void k2(const u16* __restrict__ Dskip, char* __restrict__ ws) {
    extern __shared__ __attribute__((aligned(16))) char smem[];
    STAMP_BEGIN
    const int bid = blockIdx.x;
    if (bid >= 128) {
        conv_role(smem, bid - 128, (const u16*)(ws + WS_UT), (const float*)(ws + WS_KP), Dskip, (u16*)(ws + WS_AT));
    } else {
        o_role(smem, bid & 7, bid >> 3, (const u16*)(ws + WS_PQ), (const float*)(ws + WS_KVP), (u16*)(ws + WS_O));
    }
    STAMP_END(ws, 1)
}

constexpr int G3_A = 12288, G3_STG = G3_A + 8192;
constexpr int G3_NS = 7;
constexpr int K3_LDS = G3_NS * G3_STG;

DI void k3_body(char* smem, const u16* __restrict__ wo, const u16* __restrict__ o, const u16* __restrict__ aT,
                const float* __restrict__ g, u16* __restrict__ out) {
    const int tid = threadIdx.x, wave = tid >> 6, lane = tid & 63;
    FS_DECL
    FS(0)
    const int xcd = blockIdx.x & 7, idx = blockIdx.x >> 3;
    const int tile = xcd * 27 + (xcd < 4 ? xcd : 4) + idx;
    const int rt = tile / 10, cg = tile - rt * 10;
    const int row0 = rt * 96, col0 = cg * 64;
    const int wm = wave >> 1, wn = wave & 1;
    const int li = lane & 15, kg = lane >> 4, q = li >> 2, p = lane & 3;
    f32x4 gv[3];
    u32x2 av[3][2];
#pragma unroll
    for (int mi = 0; mi < 3; ++mi) {
        int m0 = row0 + wm * 48 + mi * 16 + 4 * kg;
        if (m0 > L - 4) m0 = L - 4;
        gv[mi] = *(const f32x4*)(g + m0);
#pragma unroll
        for (int ni = 0; ni < 2; ++ni) av[mi][ni] = *(const u32x2*)(aT + (size_t)(col0 + wn * 32 + ni * 16 + li) * L + m0);
    }
    asm volatile("" ::: "memory");
    const char* a_src[3];
#pragma unroll
    for (int it = 0; it < 3; ++it) {
        int id = it * 256 + tid, row = id >> 3, pc = id & 7, lc = pc ^ (row & 7);
        int gr = row0 + row; if (gr > L - 1) gr = L - 1;
        a_src[it] = (const char*)o + (size_t)gr * 1280 + lc * 16;
    }
    const char* b_src[2];
#pragma unroll
    for (int it = 0; it < 2; ++it) {
        int id = it * 256 + tid;
        int prow = id >> 3, pc = id & 7;
        int blk = prow >> 5, pos = prow & 31;
        pc ^= 2 * ((pos >> 1) & 3);
        int k = blk * 32 + 8 * (2 * (pos >> 4) + ((pos >> 2) & 1)) + 4 * ((pos >> 3) & 1) + (pos & 3);
        b_src[it] = (const char*)wo + (size_t)k * 1280 + (col0 + pc * 8) * 2;
    }
    auto stage = [&](int kt) {
        char* sa = smem + (kt % G3_NS) * G3_STG;
#pragma unroll
        for (int it = 0; it < 3; ++it)
            __builtin_amdgcn_global_load_lds((const unsigned*)(a_src[it] + kt * 128), (LDS unsigned*)(sa + it * 4096 + tid * 16), 16, 0, 0);
#pragma unroll
        for (int it = 0; it < 2; ++it)
            __builtin_amdgcn_global_load_lds((const unsigned*)(b_src[it] + (size_t)kt * 64 * 1280), (LDS unsigned*)(sa + G3_A + it * 4096 + tid * 16), 16, 0, 0);
    };
#pragma unroll
    for (int kt = 0; kt < G3_NS - 1; ++kt) stage(kt);
    FS(1)
    f32x4 acc[3][2];
#pragma unroll
    for (int i = 0; i < 3; ++i) { acc[i][0] = (f32x4){0.f, 0.f, 0.f, 0.f}; acc[i][1] = (f32x4){0.f, 0.f, 0.f, 0.f}; }
    const int a_off = (wm * 48 + li) * 128 + ((kg ^ (li & 7)) * 16);
    int b_off[2];
    {
        const int pos0 = 16 * (kg >> 1) + 4 * (kg & 1) + q;
#pragma unroll
        for (int ni = 0; ni < 2; ++ni) b_off[ni] = G3_A + pos0 * 128 + (((2 * wn + ni) ^ ((pos0 >> 1) & 3)) * 32) + p * 8;
    }
#pragma unroll
    for (int kt = 0; kt < 10; ++kt) {
        const int outst = ((9 - kt) < (G3_NS - 2) ? (9 - kt) : (G3_NS - 2)) * 5;
        asm volatile("s_waitcnt vmcnt(%0) lgkmcnt(0)\n\ts_barrier" :: "i"(outst) : "memory");
        if (kt == 0) { FS(2) }
        if (kt + G3_NS - 1 < 10) stage(kt + G3_NS - 1);
        const char* sa = smem + (kt % G3_NS) * G3_STG;
#pragma unroll
        for (int ks = 0; ks < 2; ++ks) {
            bf16x8 af[3], bfr[2];
#pragma unroll
            for (int mi = 0; mi < 3; ++mi) af[mi] = *(const bf16x8*)(sa + ((a_off + mi * 2048) ^ (ks * 64)));
#pragma unroll
            for (int ni = 0; ni < 2; ++ni) {
                s16x4 t0 = __builtin_amdgcn_ds_read_tr16_b64_v4i16((LDS s16x4*)(sa + b_off[ni] + ks * 32 * 128));
                s16x4 t1 = __builtin_amdgcn_ds_read_tr16_b64_v4i16((LDS s16x4*)(sa + b_off[ni] + (ks * 32 + 8) * 128));
                bfr[ni] = __builtin_shufflevector(t0, t1, 0, 1, 2, 3, 4, 5, 6, 7);
            }
#pragma unroll
            for (int mi = 0; mi < 3; ++mi)
#pragma unroll
                for (int ni = 0; ni < 2; ++ni)
                    acc[mi][ni] = __builtin_amdgcn_mfma_f32_16x16x32_bf16(af[mi], bfr[ni], acc[mi][ni], 0, 0, 0);
        }
    }
    FS(3)
    char* ot = smem + 3 * G3_STG;
#pragma unroll
    for (int mi = 0; mi < 3; ++mi)
#pragma unroll
        for (int ni = 0; ni < 2; ++ni) {
            float a4[4] = {lo2f(av[mi][ni][0]), hi2f(av[mi][ni][0]), lo2f(av[mi][ni][1]), hi2f(av[mi][ni][1])};
#pragma unroll
            for (int r = 0; r < 4; ++r) {
                float gg = gv[mi][r];
                float omg = rbf(1.f - gg);
                float b = rbf(acc[mi][ni][r]);
                float res = rbf(gg * a4[r]) + rbf(omg * b);
                *(u16*)(ot + (wm * 48 + mi * 16 + 4 * kg + r) * 144 + (wn * 32 + ni * 16 + li) * 2) = (u16)(pack2(res, 0.f) & 0xffffu);
            }
        }
    __syncthreads();
    FS(4)
#pragma unroll
    for (int it = 0; it < 3; ++it) {
        int id = it * 256 + tid, r = id >> 3, c = id & 7;
        u32x4 v = *(const u32x4*)(ot + r * 144 + c * 16);
        if (row0 + r < L) *(u32x4*)(out + (size_t)(row0 + r) * D + col0 + c * 8) = v;
    }
    FS(5)
    FS_OUT((char*)g - WS_G, 3)
}

__global__ __launch_bounds__(256, 1) void k3(const u16* __restrict__ wo, const char* __restrict__ ws,
                                             u16* __restrict__ out) {
    extern __shared__ __attribute__((aligned(16))) char smem[];
    STAMP_BEGIN
    k3_body(smem, wo, (const u16*)(ws + WS_O), (const u16*)(ws + WS_AT), (const float*)(ws + WS_G), out);
    STAMP_END(ws, 2)
}

extern "C" void kernel_launch(void* const* d_in, const int* in_sizes, int n_in, void* d_out, int out_size,
                              void* d_ws, size_t ws_size, hipStream_t stream) {
    const u16* u = (const u16*)d_in[0];
    const u16* logA = (const u16*)d_in[1];
    const u16* Aim = (const u16*)d_in[2];
    const u16* Cr = (const u16*)d_in[3];
    const u16* Ci = (const u16*)d_in[4];
    const u16* logdt = (const u16*)d_in[5];
    const u16* Dskip = (const u16*)d_in[6];
    const u16* wqkv = (const u16*)d_in[8];
    const u16* wo = (const u16*)d_in[9];
    const u16* gw = (const u16*)d_in[10];
    char* ws = (char*)d_ws;
    (void)hipFuncSetAttribute((const void*)k1, hipFuncAttributeMaxDynamicSharedMemorySize, K1_LDS);
    (void)hipFuncSetAttribute((const void*)k3, hipFuncAttributeMaxDynamicSharedMemorySize, K3_LDS);
    k1<<<448, 256, K1_LDS, stream>>>(u, wqkv, logA, Aim, Cr, Ci, logdt, gw, ws);
    k2<<<768, 256, K2_LDS, stream>>>(Dskip, ws);
    k3<<<220, 256, K3_LDS, stream>>>(wo, ws, (u16*)d_out);
}
```

```cpp
#include <hip/hip_runtime.h>
#include <stdint.h>
#include <type_traits>

typedef unsigned short u16;
typedef short bf16x8 __attribute__((ext_vector_type(8)));
typedef _Float16 f16x8 __attribute__((ext_vector_type(8)));
typedef _Float16 f16x2 __attribute__((ext_vector_type(2)));
typedef short s16x4 __attribute__((ext_vector_type(4)));
typedef unsigned short u16x2 __attribute__((ext_vector_type(2)));
typedef float f32x2 __attribute__((ext_vector_type(2)));
typedef float f32x4 __attribute__((ext_vector_type(4)));
typedef float f32x16 __attribute__((ext_vector_type(16)));
typedef unsigned u32x4 __attribute__((ext_vector_type(4)));
typedef unsigned u32x2 __attribute__((ext_vector_type(2)));
typedef __bf16 bfv2 __attribute__((ext_vector_type(2)));
#define LDS __attribute__((address_space(3)))
#define DI __device__ __forceinline__

DI int lane_id() { int l; asm volatile("v_mbcnt_lo_u32_b32 %0, -1, 0\n\tv_mbcnt_hi_u32_b32 %0, -1, %0" : "=v"(l)); return l; }
constexpr int L = 2048, D = 640, NS = 192, DH = 20;


DI float bf2f(u16 v) { return __uint_as_float(((unsigned)v) << 16); }
DI unsigned pack2(float a, float b) {
    f32x2 f = {a, b};
    bfv2 v = __builtin_convertvector(f, bfv2);
    return __builtin_bit_cast(unsigned, v);
}
DI float lo2f(unsigned p) { return __uint_as_float(p << 16); }
DI float hi2f(unsigned p) { return __uint_as_float(p & 0xffff0000u); }
DI float rbf(float f) { return __uint_as_float(pack2(0.f, f)); }
constexpr float PHI1 = 1.000001f;
DI float phi(float a) {
    float e = __builtin_amdgcn_fmed3f(__builtin_amdgcn_exp2f(__builtin_fmaf(a, 1.44269504088896341f, -1.44269504088896341f * PHI1)), 0.f, 1.f);
    e += 1e-6f;
    asm("v_max_f32 %0, %0, %1" : "+v"(e) : "v"(a));
    return e;
}

#define FILL 20
#define NC 512
constexpr int KT_N = 672;
constexpr size_t WS_KT = 0;
constexpr size_t WS_KS = WS_KT + (size_t)D * KT_N * 2;
constexpr size_t WS_UM = WS_KS + (size_t)D * 4;
constexpr size_t WS_UT = WS_UM + (size_t)D * 64;
constexpr size_t WS_G = WS_UT + (size_t)D * L * 2;
constexpr size_t WS_PQ = WS_G + (size_t)L * 8;
constexpr size_t WS_KVP = WS_PQ + (size_t)D * L * 2;
constexpr size_t WS_O = WS_KVP + (size_t)16 * 32 * 640 * 4;
constexpr size_t WS_AT = WS_O + (size_t)D * L * 2;
constexpr size_t WS_WT = WS_AT + (size_t)D * L * 2;
constexpr size_t WS_END = WS_WT + (size_t)D * D * 2 + 14336;
static_assert(WS_PQ % 128 == 0 && WS_UT % 128 == 0, "");
typedef __amdgpu_buffer_rsrc_t rsrc_t;
DI rsrc_t mk_rsrc(const void* p) { return __builtin_amdgcn_make_buffer_rsrc((void*)p, 0, (int)WS_END, 0x00020000); }
#define WT1 16
DI void st16(rsrc_t rs, unsigned off, u32x4 v) { __builtin_amdgcn_raw_buffer_store_b128(v, rs, off, 0, 16); }
DI void st16a(rsrc_t rs, unsigned off, u32x4 v) { __builtin_amdgcn_raw_buffer_store_b128(v, rs, off, 0, WT1); }
DI u32x4 swap32(u32x2 a, u32x2 b) {
    auto s0 = __builtin_amdgcn_permlane32_swap(a[0], b[0], false, false);
    auto s1 = __builtin_amdgcn_permlane32_swap(a[1], b[1], false, false);
    return (u32x4){s0[0], s1[0], s0[1], s1[1]};
}
DI u32x4 swap16(u32x2 a, u32x2 b) {
    auto s0 = __builtin_amdgcn_permlane16_swap(a[0], b[0], false, false);
    auto s1 = __builtin_amdgcn_permlane16_swap(a[1], b[1], false, false);
    return (u32x4){s0[0], s1[0], s0[1], s1[1]};
}

#define KPRIO 1
#define WOP 2
#define FDEF 6
#define GTRY 8
#define PS0 3
#define PS1 5
#define PS2 8
#define DEFER2 10
#define DEFER 20
#define PRIO_C 2
#define PRIO_P 3
constexpr int KG_P = 288, KG_S = 4608;
constexpr int G1_A = 16384, G1_BS = 10240, G1_NA = 4, G1_NB = 8;
constexpr int G1_BOFF = 256;
constexpr int K1_RB = G1_BOFF + G1_NB * G1_BS;
constexpr int K1_KG = G1_BOFF + 4 * G1_BS;
constexpr int K1_LDS = K1_RB + G1_NA * G1_A + 4608;
constexpr int F_WO = G1_BOFF + 6 * KG_S, F_KT = F_WO + 9216, F_CV = K1_KG + 6 * KG_S;
static_assert(F_KT + 2 * 1408 <= K1_KG, "");
static_assert(K1_KG + 5 * KG_S < 65536, "");
static_assert(K1_RB % 128 == 0 && K1_LDS <= 163840, "");

DI unsigned lds_u32(const char* p) { return (unsigned)(size_t)(LDS const char*)p; }
DI void flag_set(unsigned addr, unsigned v) { asm volatile("ds_write_b32 %0, %1" :: "v"(addr), "v"(v) : "memory"); }
DI void flag_wait(unsigned addr, unsigned need) {
    unsigned v, cnt = 1u << 20;
    asm volatile("1:\n\tds_read_b32 %0, %2\n\ts_waitcnt lgkmcnt(0)\n\tv_cmp_gt_u32 vcc, %3, %0\n\ts_cbranch_vccz 2f\n\t"
                 "s_sleep 1\n\ts_sub_u32 %1, %1, 1\n\ts_cbranch_scc0 1b\n2:"
                 : "=&v"(v), "+s"(cnt) : "v"(addr), "s"(need) : "memory", "vcc", "scc");
}
#define VMW(n) asm volatile("s_waitcnt vmcnt(%0)" :: "i"(n) : "memory")

DI void dma4(unsigned dst, const char* src, const unsigned* v) {
    asm volatile("s_mov_b32 m0, %0\n\ts_nop 0\n\t"
                 "global_load_lds_dwordx4 %2, %1\n\tglobal_load_lds_dwordx4 %3, %1 offset:1024\n\t"
                 "global_load_lds_dwordx4 %4, %1 offset:2048\n\tglobal_load_lds_dwordx4 %5, %1 offset:3072"
                 :: "s"(dst), "s"(src), "v"(v[0]), "v"(v[1]), "v"(v[2]), "v"(v[3]) : "memory");
}
DI void dma1(unsigned dst, const char* src, unsigned v) {
    asm volatile("s_mov_b32 m0, %0\n\ts_nop 0\n\tglobal_load_lds_dwordx4 %2, %1" :: "s"(dst), "s"(src), "v"(v) : "memory");
}
DI void dma_flag(unsigned dst, const char* tab, unsigned v4) {
    asm volatile("s_mov_b32 m0, %0\n\ts_mov_b64 exec, 1\n\tglobal_load_lds_dword %2, %1\n\ts_mov_b64 exec, -1" :: "s"(dst), "s"(tab), "v"(v4) : "memory");
}
DI void producer(char* smem, const u16* __restrict__ x, const u16* __restrict__ W, int rt, int cg, bool isq, int pw, const char* tab, char* ws) {
    const int lane = threadIdx.x & 63;
    const int half = pw & 1;
    const unsigned sb = lds_u32(smem);
    const unsigned fp = sb + 4 * pw, ff = sb + 16 + 4 * (lane & 3);
    unsigned v[5];
    if (pw >= 2) {
#pragma unroll
        for (int it = 0; it < 4; ++it)
            v[it] = 4096 + (lane >> 3) * 1280 + it * 9216 + (((lane & 7) ^ (lane >> 4) ^ (4 * (it & 1))) * 16);
        const char* sa = (const char*)x + (size_t)(rt * 128 + 64 * half) * 1280 - 4096;
        const unsigned da = sb + K1_RB + half * 8192;
        dma4(da, sa, v); dma4(da + 4096, sa + 40960, v);
        asm volatile("s_barrier" ::: "memory");
        dma_flag(fp, tab, 8);
#pragma unroll 1
        for (int k = 1; k < 10; ++k) {
            if (k >= G1_NA) flag_wait(ff, 2 * (k - G1_NA) + 2);
            dma4(da + (k & (G1_NA - 1)) * G1_A, sa + k * 128, v);
            dma4(da + (k & (G1_NA - 1)) * G1_A + 4096, sa + k * 128 + 40960, v);
            dma_flag(fp, tab, k < 9 ? 8 * k + 8 : 96);
        }
    } else {
        const int e = lane & 3;
        const unsigned c4 = isq ? 256 + 16 * e : (e >> 1) * 1280 + ((cg & 1) ? 0 : 128) + (e & 1) * 16;
        const unsigned cs = isq ? 64 : 1280, c0 = 16 * e + (isq ? 0 : 32 * (cg & 1)), c2 = isq ? 128 : 64;
#pragma unroll
        for (int it = 0; it < 5; ++it) {
            const int qd = 16 * it + (lane >> 2), row = (qd * 205) >> 10, t = qd - 5 * row;
            v[it] = 4096 + row * 3840 - (it < 4 ? it * 1024 : 0) + (t == 4 ? c4 : c0 + (t & 1) * cs + (t >> 1) * c2);
        }
        const char* sw = (const char*)W + (isq ? 320 * cg : 1280 + 160 * cg) - 4096;
        const unsigned db = sb + G1_BOFF;
        dma4(db + half * 5120, sw + half * 61440, v); dma1(db + half * 5120 + 4096, sw + half * 61440, v[4]);
        asm volatile("s_barrier" ::: "memory");
        dma_flag(fp, tab, 4 + 4 * half);
#pragma unroll 1
        for (int h = 1 + half; h < 20; h += 2) {
            if (h >= G1_NB) flag_wait(ff, h - G1_NB + 1);
            const unsigned d = db + (h & (G1_NB - 1)) * G1_BS;
            const char* s = sw + (size_t)h * 122880;
            dma4(d, s, v); dma1(d + 4096, s, v[4]);
            dma4(d + 5120, s + 61440, v); dma1(d + 9216, s + 61440, v[4]);
            dma_flag(fp, tab, h < 18 ? 4 * h + 8 : 96);
        }
    }
    VMW(0);
}
static_assert(G1_NA == 4 && G1_NB == 8, "");

template <bool IS_Q>
DI void gemm_consumer(char* smem, int wave, int rt, int cg, char* __restrict__ ws, const char* tab) {
    const int rb = wave >> 1, cb = wave & 1;
    f32x16 av[5];
#pragma unroll
    for (int j = 0; j < 5; ++j)
#pragma unroll
        for (int i = 0; i < 16; ++i) av[j][i] = IS_Q || j == 0 || j == 3 ? PHI1 : (j == 2 && (lane_id() & 16) == 0 ? PHI1 : 0.f);
    const unsigned sbase = lds_u32(smem);
    {
        const int lane = lane_id();
        const unsigned fa = sbase + 4 * (lane & 3), fw = sbase + 16 + 4 * wave;
        const int r = lane & 31, h = lane >> 5;
        unsigned tA0 = sbase + K1_RB + (64 * rb + 32 * cb + r) * 128 + ((h ^ ((r >> 1) & 7)) * 16), tA1 = tA0 + 4096 - 8192 * cb;
        unsigned tBX = sbase + G1_BOFF + (8 * h + ((lane & 15) >> 2)) * 320 + ((lane >> 4) & 1) * 32 + (lane & 3) * 8;
        unsigned tB = tBX + 128 * cb;
        tBX += 256;
        if (IS_Q) {
            asm volatile(
        "s_mov_b32 s40, 0\n"
        "s_mov_b32 s41, 0\n"
        "s_mov_b32 s42, 1\n"
        "s_mov_b32 s44, 20480\n"
        "s_mov_b32 s45, -61440\n"
        "s_mov_b32 s46, 16384\n"
        "s_mov_b32 s47, -49152\n"
        "s_mov_b32 s48, 0x400000\n"
        "v_mov_b32 v121, 4\n"
        "s_mov_b32 m0, %11\n"
        "5:\n"
        "ds_read_b32 v120, %9\n"
        "s_sub_u32 s48, s48, 1\n"
        "s_cbranch_scc1 4f\n"
        "s_waitcnt lgkmcnt(0)\n"
        "v_cmp_gt_u32 vcc, s42, v120\n"
        "s_cbranch_vccnz 5b\n"
        "4:\n"
        "ds_read_b64_tr_b16 v[96:97], %8 offset:0\n"
        "ds_read_b64_tr_b16 v[98:99], %8 offset:1280\n"
        "ds_read_b128 v[80:83], %5\n"
        "v_xor_b32 %5, 32, %5\n"
        "ds_read_b64_tr_b16 v[92:93], %7 offset:64\n"
        "ds_read_b64_tr_b16 v[94:95], %7 offset:1344\n"
        "ds_read_b128 v[84:87], %6\n"
        "v_xor_b32 %6, 32, %6\n"
        "ds_read_b64_tr_b16 v[88:89], %7 offset:0\n"
        "ds_read_b64_tr_b16 v[90:91], %7 offset:1280\n"
        "ds_read_b64_tr_b16 v[116:117], %8 offset:5120\n"
        "ds_read_b64_tr_b16 v[118:119], %8 offset:6400\n"
        "ds_read_b32 v120, %9\n"
        "s_add_u32 s42, s42, 1\n"
        "ds_read_b128 v[100:103], %5\n"
        "v_xor_b32 %5, 64, %5\n"
        "ds_read_b64_tr_b16 v[112:113], %7 offset:5184\n"
        "ds_read_b64_tr_b16 v[114:115], %7 offset:6464\n"
        "ds_read_b128 v[104:107], %6\n"
        "v_xor_b32 %6, 64, %6\n"
        "ds_read_b64_tr_b16 v[108:109], %7 offset:5120\n"
        "ds_read_b64_tr_b16 v[110:111], %7 offset:6400\n"
        "1:\n"
        "s_waitcnt lgkmcnt(14)\n"
        "v_mfma_f32_32x32x16_bf16 v[32:47], v[96:99], v[80:83], v[32:47]\n"
        "s_waitcnt lgkmcnt(6)\n"
        "v_cmp_gt_u32 vcc, s42, v120\n"
        "s_cbranch_vccnz 6f\n"
        "2:\n"
        "ds_read_b64_tr_b16 v[96:97], %8 offset:15360\n"
        "ds_read_b64_tr_b16 v[98:99], %8 offset:16640\n"
        "v_mfma_f32_32x32x16_bf16 v[0:15], v[88:91], v[80:83], v[0:15]\n"
        "s_add_u32 s40, s40, 1\n"
        "s_cmp_eq_u32 s40, 4\n"
        "s_cselect_b32 s43, s47, s46\n"
        "s_cselect_b32 s40, 0, s40\n"
        "v_mfma_f32_32x32x16_bf16 v[16:31], v[92:95], v[80:83], v[16:31]\n"
        "s_add_u32 s41, s41, 1\n"
        "s_cmp_eq_u32 s41, 4\n"
        "s_cselect_b32 s49, s45, s44\n"
        "s_cselect_b32 s41, 0, s41\n"
        "ds_read_b128 v[80:83], %5\n"
        "v_xor_b32 %5, 32, %5\n"
        "v_mfma_f32_32x32x16_bf16 v[64:79], v[92:95], v[84:87], v[64:79]\n"
        "ds_read_b64_tr_b16 v[92:93], %7 offset:15424\n"
        "ds_read_b64_tr_b16 v[94:95], %7 offset:16704\n"
        "v_mfma_f32_32x32x16_bf16 v[48:63], v[88:91], v[84:87], v[48:63]\n"
        "ds_read_b128 v[84:87], %6\n"
        "v_xor_b32 %6, 32, %6\n"
        "ds_read_b64_tr_b16 v[88:89], %7 offset:15360\n"
        "ds_read_b64_tr_b16 v[90:91], %7 offset:16640\n"
        "s_waitcnt lgkmcnt(13)\n"
        "v_mfma_f32_32x32x16_bf16 v[32:47], v[116:119], v[100:103], v[32:47]\n"
        "ds_read_b64_tr_b16 v[116:117], %8 offset:10240\n"
        "ds_read_b64_tr_b16 v[118:119], %8 offset:11520\n"
        "s_waitcnt lgkmcnt(10)\n"
        "v_mfma_f32_32x32x16_bf16 v[0:15], v[108:111], v[100:103], v[0:15]\n"
        "ds_read_b32 v120, %9\n"
        "s_add_u32 s42, s42, 1\n"
        "s_mov_b64 exec, 1\n"
        "global_load_lds_dword v121, %10\n"
        "s_mov_b64 exec, -1\n"
        "v_add_u32 v121, 4, v121\n"
        "v_mfma_f32_32x32x16_bf16 v[16:31], v[112:115], v[100:103], v[16:31]\n"
        "ds_read_b128 v[100:103], %5\n"
        "v_xad_u32 %5, %5, 64, s43\n"
        "v_mfma_f32_32x32x16_bf16 v[64:79], v[112:115], v[104:107], v[64:79]\n"
        "ds_read_b64_tr_b16 v[112:113], %7 offset:10304\n"
        "ds_read_b64_tr_b16 v[114:115], %7 offset:11584\n"
        "v_mfma_f32_32x32x16_bf16 v[48:63], v[108:111], v[104:107], v[48:63]\n"
        "ds_read_b128 v[104:107], %6\n"
        "v_xad_u32 %6, %6, 64, s43\n"
        "ds_read_b64_tr_b16 v[108:109], %7 offset:10240\n"
        "ds_read_b64_tr_b16 v[110:111], %7 offset:11520\n"
        "s_waitcnt lgkmcnt(14)\n"
        "v_mfma_f32_32x32x16_bf16 v[32:47], v[96:99], v[80:83], v[32:47]\n"
        "s_waitcnt lgkmcnt(6)\n"
        "v_cmp_gt_u32 vcc, s42, v120\n"
        "s_cbranch_vccnz 7f\n"
        "3:\n"
        "v_add_u32 %8, s49, %8\n"
        "ds_read_b64_tr_b16 v[96:97], %8 offset:0\n"
        "ds_read_b64_tr_b16 v[98:99], %8 offset:1280\n"
        "v_mfma_f32_32x32x16_bf16 v[0:15], v[88:91], v[80:83], v[0:15]\n"
        "v_add_u32 %7, s49, %7\n"
        "v_mfma_f32_32x32x16_bf16 v[16:31], v[92:95], v[80:83], v[16:31]\n"
        "ds_read_b128 v[80:83], %5\n"
        "v_xor_b32 %5, 32, %5\n"
        "v_mfma_f32_32x32x16_bf16 v[64:79], v[92:95], v[84:87], v[64:79]\n"
        "ds_read_b64_tr_b16 v[92:93], %7 offset:64\n"
        "ds_read_b64_tr_b16 v[94:95], %7 offset:1344\n"
        "v_mfma_f32_32x32x16_bf16 v[48:63], v[88:91], v[84:87], v[48:63]\n"
        "ds_read_b128 v[84:87], %6\n"
        "v_xor_b32 %6, 32, %6\n"
        "ds_read_b64_tr_b16 v[88:89], %7 offset:0\n"
        "ds_read_b64_tr_b16 v[90:91], %7 offset:1280\n"
        "s_waitcnt lgkmcnt(13)\n"
        "v_mfma_f32_32x32x16_bf16 v[32:47], v[116:119], v[100:103], v[32:47]\n"
        "ds_read_b64_tr_b16 v[116:117], %8 offset:5120\n"
        "ds_read_b64_tr_b16 v[118:119], %8 offset:6400\n"
        "s_waitcnt lgkmcnt(10)\n"
        "v_mfma_f32_32x32x16_bf16 v[0:15], v[108:111], v[100:103], v[0:15]\n"
        "ds_read_b32 v120, %9\n"
        "s_add_u32 s42, s42, 1\n"
        "s_mov_b64 exec, 1\n"
        "global_load_lds_dword v121, %10\n"
        "s_mov_b64 exec, -1\n"
        "v_add_u32 v121, 4, v121\n"
        "v_mfma_f32_32x32x16_bf16 v[16:31], v[112:115], v[100:103], v[16:31]\n"
        "ds_read_b128 v[100:103], %5\n"
        "v_xor_b32 %5, 64, %5\n"
        "v_mfma_f32_32x32x16_bf16 v[64:79], v[112:115], v[104:107], v[64:79]\n"
        "ds_read_b64_tr_b16 v[112:113], %7 offset:5184\n"
        "ds_read_b64_tr_b16 v[114:115], %7 offset:6464\n"
        "v_mfma_f32_32x32x16_bf16 v[48:63], v[108:111], v[104:107], v[48:63]\n"
        "ds_read_b128 v[104:107], %6\n"
        "v_xor_b32 %6, 64, %6\n"
        "ds_read_b64_tr_b16 v[108:109], %7 offset:5120\n"
        "ds_read_b64_tr_b16 v[110:111], %7 offset:6400\n"
        "s_cmp_lg_u32 s42, 22\n"
        "s_cbranch_scc1 1b\n"
        "s_branch 9f\n"
        "6:\n"
        "ds_read_b32 v120, %9\n"
        "s_sub_u32 s48, s48, 1\n"
        "s_cbranch_scc1 2b\n"
        "s_waitcnt lgkmcnt(0)\n"
        "v_cmp_gt_u32 vcc, s42, v120\n"
        "s_cbranch_vccnz 6b\n"
        "s_branch 2b\n"
        "7:\n"
        "ds_read_b32 v120, %9\n"
        "s_sub_u32 s48, s48, 1\n"
        "s_cbranch_scc1 3b\n"
        "s_waitcnt lgkmcnt(0)\n"
        "v_cmp_gt_u32 vcc, s42, v120\n"
        "s_cbranch_vccnz 7b\n"
        "s_branch 3b\n"
        "9:\n"
        "s_waitcnt lgkmcnt(0)\n"
        "s_nop 15\n"
        "s_nop 15\n"
        : "+{v[0:15]}"(av[0]), "+{v[16:31]}"(av[1]), "+{v[32:47]}"(av[2]), "+{v[48:63]}"(av[3]), "+{v[64:79]}"(av[4]), "+v"(tA0), "+v"(tA1), "+v"(tB), "+v"(tBX)
        : "v"(fa), "s"(tab), "s"(fw)
        : "memory", "scc", "vcc", "s40", "s41", "s42", "s43", "s44", "s45", "s46", "s47", "s48", "s49", "v80", "v81", "v82", "v83", "v84", "v85", "v86", "v87", "v88", "v89", "v90", "v91", "v92", "v93", "v94", "v95", "v96", "v97", "v98", "v99", "v100", "v101", "v102", "v103", "v104", "v105", "v106", "v107", "v108", "v109", "v110", "v111", "v112", "v113", "v114", "v115", "v116", "v117", "v118", "v119", "v120", "v121");
        } else {
            asm volatile(
        "s_mov_b32 s40, 0\n"
        "s_mov_b32 s41, 0\n"
        "s_mov_b32 s42, 1\n"
        "s_mov_b32 s44, 20480\n"
        "s_mov_b32 s45, -61440\n"
        "s_mov_b32 s46, 16384\n"
        "s_mov_b32 s47, -49152\n"
        "s_mov_b32 s48, 0x400000\n"
        "v_mov_b32 v121, 4\n"
        "s_mov_b32 m0, %11\n"
        "5:\n"
        "ds_read_b32 v120, %9\n"
        "s_sub_u32 s48, s48, 1\n"
        "s_cbranch_scc1 4f\n"
        "s_waitcnt lgkmcnt(0)\n"
        "v_cmp_gt_u32 vcc, s42, v120\n"
        "s_cbranch_vccnz 5b\n"
        "4:\n"
        "ds_read_b64_tr_b16 v[96:97], %8 offset:0\n"
        "ds_read_b64_tr_b16 v[98:99], %8 offset:1280\n"
        "ds_read_b128 v[80:83], %5\n"
        "v_xor_b32 %5, 32, %5\n"
        "ds_read_b64_tr_b16 v[92:93], %7 offset:64\n"
        "ds_read_b64_tr_b16 v[94:95], %7 offset:1344\n"
        "ds_read_b128 v[84:87], %6\n"
        "v_xor_b32 %6, 32, %6\n"
        "ds_read_b64_tr_b16 v[88:89], %7 offset:0\n"
        "ds_read_b64_tr_b16 v[90:91], %7 offset:1280\n"
        "ds_read_b64_tr_b16 v[116:117], %8 offset:5120\n"
        "ds_read_b64_tr_b16 v[118:119], %8 offset:6400\n"
        "ds_read_b32 v120, %9\n"
        "s_add_u32 s42, s42, 1\n"
        "ds_read_b128 v[100:103], %5\n"
        "v_xor_b32 %5, 64, %5\n"
        "ds_read_b64_tr_b16 v[112:113], %7 offset:5184\n"
        "ds_read_b64_tr_b16 v[114:115], %7 offset:6464\n"
        "ds_read_b128 v[104:107], %6\n"
        "v_xor_b32 %6, 64, %6\n"
        "ds_read_b64_tr_b16 v[108:109], %7 offset:5120\n"
        "ds_read_b64_tr_b16 v[110:111], %7 offset:6400\n"
        "1:\n"
        "s_waitcnt lgkmcnt(14)\n"
        "v_mfma_f32_32x32x16_bf16 v[32:47], v[80:83], v[96:99], v[32:47]\n"
        "s_waitcnt lgkmcnt(6)\n"
        "v_cmp_gt_u32 vcc, s42, v120\n"
        "s_cbranch_vccnz 6f\n"
        "2:\n"
        "ds_read_b64_tr_b16 v[96:97], %8 offset:15360\n"
        "ds_read_b64_tr_b16 v[98:99], %8 offset:16640\n"
        "v_mfma_f32_32x32x16_bf16 v[0:15], v[80:83], v[88:91], v[0:15]\n"
        "s_add_u32 s40, s40, 1\n"
        "s_cmp_eq_u32 s40, 4\n"
        "s_cselect_b32 s43, s47, s46\n"
        "s_cselect_b32 s40, 0, s40\n"
        "v_mfma_f32_32x32x16_bf16 v[16:31], v[80:83], v[92:95], v[16:31]\n"
        "s_add_u32 s41, s41, 1\n"
        "s_cmp_eq_u32 s41, 4\n"
        "s_cselect_b32 s49, s45, s44\n"
        "s_cselect_b32 s41, 0, s41\n"
        "ds_read_b128 v[80:83], %5\n"
        "v_xor_b32 %5, 32, %5\n"
        "v_mfma_f32_32x32x16_bf16 v[64:79], v[84:87], v[92:95], v[64:79]\n"
        "ds_read_b64_tr_b16 v[92:93], %7 offset:15424\n"
        "ds_read_b64_tr_b16 v[94:95], %7 offset:16704\n"
        "v_mfma_f32_32x32x16_bf16 v[48:63], v[84:87], v[88:91], v[48:63]\n"
        "ds_read_b128 v[84:87], %6\n"
        "v_xor_b32 %6, 32, %6\n"
        "ds_read_b64_tr_b16 v[88:89], %7 offset:15360\n"
        "ds_read_b64_tr_b16 v[90:91], %7 offset:16640\n"
        "s_waitcnt lgkmcnt(13)\n"
        "v_mfma_f32_32x32x16_bf16 v[32:47], v[100:103], v[116:119], v[32:47]\n"
        "ds_read_b64_tr_b16 v[116:117], %8 offset:10240\n"
        "ds_read_b64_tr_b16 v[118:119], %8 offset:11520\n"
        "s_waitcnt lgkmcnt(10)\n"
        "v_mfma_f32_32x32x16_bf16 v[0:15], v[100:103], v[108:111], v[0:15]\n"
        "ds_read_b32 v120, %9\n"
        "s_add_u32 s42, s42, 1\n"
        "s_mov_b64 exec, 1\n"
        "global_load_lds_dword v121, %10\n"
        "s_mov_b64 exec, -1\n"
        "v_add_u32 v121, 4, v121\n"
        "v_mfma_f32_32x32x16_bf16 v[16:31], v[100:103], v[112:115], v[16:31]\n"
        "ds_read_b128 v[100:103], %5\n"
        "v_xad_u32 %5, %5, 64, s43\n"
        "v_mfma_f32_32x32x16_bf16 v[64:79], v[104:107], v[112:115], v[64:79]\n"
        "ds_read_b64_tr_b16 v[112:113], %7 offset:10304\n"
        "ds_read_b64_tr_b16 v[114:115], %7 offset:11584\n"
        "v_mfma_f32_32x32x16_bf16 v[48:63], v[104:107], v[108:111], v[48:63]\n"
        "ds_read_b128 v[104:107], %6\n"
        "v_xad_u32 %6, %6, 64, s43\n"
        "ds_read_b64_tr_b16 v[108:109], %7 offset:10240\n"
        "ds_read_b64_tr_b16 v[110:111], %7 offset:11520\n"
        "s_waitcnt lgkmcnt(14)\n"
        "v_mfma_f32_32x32x16_bf16 v[32:47], v[80:83], v[96:99], v[32:47]\n"
        "s_waitcnt lgkmcnt(6)\n"
        "v_cmp_gt_u32 vcc, s42, v120\n"
        "s_cbranch_vccnz 7f\n"
        "3:\n"
        "v_add_u32 %8, s49, %8\n"
        "ds_read_b64_tr_b16 v[96:97], %8 offset:0\n"
        "ds_read_b64_tr_b16 v[98:99], %8 offset:1280\n"
        "v_mfma_f32_32x32x16_bf16 v[0:15], v[80:83], v[88:91], v[0:15]\n"
        "v_add_u32 %7, s49, %7\n"
        "v_mfma_f32_32x32x16_bf16 v[16:31], v[80:83], v[92:95], v[16:31]\n"
        "ds_read_b128 v[80:83], %5\n"
        "v_xor_b32 %5, 32, %5\n"
        "v_mfma_f32_32x32x16_bf16 v[64:79], v[84:87], v[92:95], v[64:79]\n"
        "ds_read_b64_tr_b16 v[92:93], %7 offset:64\n"
        "ds_read_b64_tr_b16 v[94:95], %7 offset:1344\n"
        "v_mfma_f32_32x32x16_bf16 v[48:63], v[84:87], v[88:91], v[48:63]\n"
        "ds_read_b128 v[84:87], %6\n"
        "v_xor_b32 %6, 32, %6\n"
        "ds_read_b64_tr_b16 v[88:89], %7 offset:0\n"
        "ds_read_b64_tr_b16 v[90:91], %7 offset:1280\n"
        "s_waitcnt lgkmcnt(13)\n"
        "v_mfma_f32_32x32x16_bf16 v[32:47], v[100:103], v[116:119], v[32:47]\n"
        "ds_read_b64_tr_b16 v[116:117], %8 offset:5120\n"
        "ds_read_b64_tr_b16 v[118:119], %8 offset:6400\n"
        "s_waitcnt lgkmcnt(10)\n"
        "v_mfma_f32_32x32x16_bf16 v[0:15], v[100:103], v[108:111], v[0:15]\n"
        "ds_read_b32 v120, %9\n"
        "s_add_u32 s42, s42, 1\n"
        "s_mov_b64 exec, 1\n"
        "global_load_lds_dword v121, %10\n"
        "s_mov_b64 exec, -1\n"
        "v_add_u32 v121, 4, v121\n"
        "v_mfma_f32_32x32x16_bf16 v[16:31], v[100:103], v[112:115], v[16:31]\n"
        "ds_read_b128 v[100:103], %5\n"
        "v_xor_b32 %5, 64, %5\n"
        "v_mfma_f32_32x32x16_bf16 v[64:79], v[104:107], v[112:115], v[64:79]\n"
        "ds_read_b64_tr_b16 v[112:113], %7 offset:5184\n"
        "ds_read_b64_tr_b16 v[114:115], %7 offset:6464\n"
        "v_mfma_f32_32x32x16_bf16 v[48:63], v[104:107], v[108:111], v[48:63]\n"
        "ds_read_b128 v[104:107], %6\n"
        "v_xor_b32 %6, 64, %6\n"
        "ds_read_b64_tr_b16 v[108:109], %7 offset:5120\n"
        "ds_read_b64_tr_b16 v[110:111], %7 offset:6400\n"
        "s_cmp_lg_u32 s42, 22\n"
        "s_cbranch_scc1 1b\n"
        "s_branch 9f\n"
        "6:\n"
        "ds_read_b32 v120, %9\n"
        "s_sub_u32 s48, s48, 1\n"
        "s_cbranch_scc1 2b\n"
        "s_waitcnt lgkmcnt(0)\n"
        "v_cmp_gt_u32 vcc, s42, v120\n"
        "s_cbranch_vccnz 6b\n"
        "s_branch 2b\n"
        "7:\n"
        "ds_read_b32 v120, %9\n"
        "s_sub_u32 s48, s48, 1\n"
        "s_cbranch_scc1 3b\n"
        "s_waitcnt lgkmcnt(0)\n"
        "v_cmp_gt_u32 vcc, s42, v120\n"
        "s_cbranch_vccnz 7b\n"
        "s_branch 3b\n"
        "9:\n"
        "s_waitcnt lgkmcnt(0)\n"
        "s_nop 15\n"
        "s_nop 15\n"
        : "+{v[0:15]}"(av[0]), "+{v[16:31]}"(av[1]), "+{v[32:47]}"(av[2]), "+{v[48:63]}"(av[3]), "+{v[64:79]}"(av[4]), "+v"(tA0), "+v"(tA1), "+v"(tB), "+v"(tBX)
        : "v"(fa), "s"(tab), "s"(fw)
        : "memory", "scc", "vcc", "s40", "s41", "s42", "s43", "s44", "s45", "s46", "s47", "s48", "s49", "v80", "v81", "v82", "v83", "v84", "v85", "v86", "v87", "v88", "v89", "v90", "v91", "v92", "v93", "v94", "v95", "v96", "v97", "v98", "v99", "v100", "v101", "v102", "v103", "v104", "v105", "v106", "v107", "v108", "v109", "v110", "v111", "v112", "v113", "v114", "v115", "v116", "v117", "v118", "v119", "v120", "v121");
        }
    }
    const int lane = lane_id();
    const int r = lane & 31, h = lane >> 5;
    if (IS_Q) {
        const rsrc_t rs = mk_rsrc(ws);
        const unsigned p0 = (unsigned)WS_PQ + ((20 * cg + 8 * cb + h) * L + rt * 128 + 64 * rb + 32 * cb + r) * 16;
        const unsigned p1 = p0 + 512 - 1024 * cb, px = p0 + (16 - 8 * cb) * L * 16;
#pragma unroll
        for (int tl = 0; tl < 5; ++tl)
#pragma unroll
            for (int g = 0; g < 4; g += 2) {
                u32x2 v, w;
                v[0] = pack2(phi(av[tl][4 * g]), phi(av[tl][4 * g + 1]));
                v[1] = pack2(phi(av[tl][4 * g + 2]), phi(av[tl][4 * g + 3]));
                w[0] = pack2(phi(av[tl][4 * g + 4]), phi(av[tl][4 * g + 5]));
                w[1] = pack2(phi(av[tl][4 * g + 6]), phi(av[tl][4 * g + 7]));
                st16a(rs, (tl == 2 ? px : (tl < 3 ? p0 : p1)) + ((tl == 1 || tl == 4 ? 4 : 0) + g) * L * 16, swap32(v, w));
            }
    } else {
        const unsigned fo = sbase + 16 + 4 * (lane & 3);
        char* T = smem + K1_RB;
        u32x2 tv[5][4];
#pragma unroll
        for (int i = 0; i < 8; ++i) {
            auto s = __builtin_amdgcn_permlane16_swap(__float_as_uint(av[2][i]), __float_as_uint(av[2][i + 8]), false, false);
            av[2][i] = __uint_as_float(s[0]); av[2][i + 8] = __uint_as_float(s[1]);
        }
#pragma unroll
        for (int tl = 0; tl < 5; ++tl)
#pragma unroll
            for (int g = 0; g < 4; ++g)
#pragma unroll
                for (int e = 0; e < 2; ++e) {
                    const float a0 = av[tl][4 * g + 2 * e], a1 = av[tl][4 * g + 2 * e + 1];
                    if (tl == 0 || tl == 3 || (tl == 2 && g < 2)) tv[tl][g][e] = pack2(phi(a0), phi(a1));
                    else tv[tl][g][e] = pack2(a0, a1);
                }
        flag_wait(fo, 20);
        char* t0 = T + (16 * (cg & 1) + 32 * cb + r) * 272 + (64 * rb + 4 * h) * 2;
        char* tx = T + (((cg & 1) ? 0 : 64) + (r & 15)) * 272 + (r >> 4) * 32 + (64 * rb + 32 * cb + 4 * h) * 2;
#pragma unroll
        for (int g = 0; g < 4; ++g) {
            *(u32x2*)(t0 + 64 * cb + 16 * g) = tv[0][g];
            *(u32x2*)(t0 + 64 * cb + 80 * 272 + 16 * g) = tv[1][g];
            *(u32x2*)(tx + 16 * (g & 1) + (g >> 1) * 80 * 272) = tv[2][g];
            *(u32x2*)(t0 + 64 * (cb ^ 1) + 16 * g) = tv[3][g];
            *(u32x2*)(t0 + 64 * (cb ^ 1) + 80 * 272 + 16 * g) = tv[4][g];
        }
        flag_set(sbase + 32 + 4 * wave, 1);
        flag_wait(fo + 16, 1);
        f32x16 kacc;
#pragma unroll
        for (int i = 0; i < 16; ++i) kacc[i] = 0.f;
        const bf16x8 ones = {0x3f80, 0x3f80, 0x3f80, 0x3f80, 0x3f80, 0x3f80, 0x3f80, 0x3f80};
#pragma unroll
        for (int s = 0; s < 8; ++s) {
            bf16x8 ka = *(const bf16x8*)(T + (20 * wave + r) * 272 + (16 * s + 8 * h) * 2);
            bf16x8 vb = *(const bf16x8*)(T + (80 + 20 * wave + r) * 272 + (16 * s + 8 * h) * 2);
            if (r == 20) vb = ones;
            kacc = __builtin_amdgcn_mfma_f32_32x32x16_bf16(ka, vb, kacc, 0, 0, 0);
        }
        const rsrc_t rs = mk_rsrc(ws);
        const unsigned kb = (unsigned)WS_KVP + (rt * 32 + cg * 4 + wave) * 2560;
#pragma unroll
        for (int q = 0; q < 2; ++q)
            st16a(rs, kb + q * 1024 + r * 32 + h * 16, __builtin_bit_cast(u32x4, (f32x4){kacc[4 * q], kacc[4 * q + 1], kacc[4 * q + 2], kacc[4 * q + 3]}));
        if (h == 0) st16a(rs, kb + 2048 + r * 16, __builtin_bit_cast(u32x4, (f32x4){kacc[8], kacc[9], kacc[10], kacc[11]}));
    }
}


constexpr int C_CS = 4160;
constexpr int C_KI = 4 * C_CS;
constexpr int C_RED = C_KI + 4608;
constexpr int C_END = C_RED + 16384;
DI float sadd(float a, float b) { float c = a + b; asm("" : "+v"(c)); return c; }
DI float smul(float a, float b) { float c = a * b; asm("" : "+v"(c)); return c; }
DI float sfma(float a, float b, float c) { float d = __builtin_fmaf(a, b, c); asm("" : "+v"(d)); return d; }
DI f32x4 add4(f32x4 a, f32x4 b) { return (f32x4){sadd(a[0], b[0]), sadd(a[1], b[1]), sadd(a[2], b[2]), sadd(a[3], b[3])}; }
DI unsigned pm(unsigned a, unsigned b, unsigned sel) { return __builtin_amdgcn_perm(a, b, sel); }
DI unsigned bf2h(unsigned p, int sc) {
    return __builtin_bit_cast(unsigned, __builtin_amdgcn_cvt_pkrtz(__builtin_amdgcn_ldexpf(lo2f(p), sc), __builtin_amdgcn_ldexpf(hi2f(p), sc)));
}
DI void cv_issue(u32x2& a0, u32x2& a1, u32x4& b, unsigned ap, unsigned bp, int k) {
    asm volatile("ds_read_b64 %0, %3 offset:%5\n\tds_read_b64 %1, %3 offset:%6\n\tds_read_b128 %2, %4 offset:%5"
                 : "=&v"(a0), "=&v"(a1), "=&v"(b) : "v"(ap), "v"(bp), "i"(k * 512), "i"(k * 512 + 8));
}
DI f16x8 cv_af(u32x2 a0, u32x2 a1) { u32x4 x = {a0[0], a0[1], a1[0], a1[1]}; return __builtin_bit_cast(f16x8, x); }
constexpr unsigned NONE = 0xffffffffu;
DI u32x4 clean(u32x4 v) { return (u32x4){min(v[0], NONE - 1), min(v[1], NONE - 1), min(v[2], NONE - 1), min(v[3], NONE - 1)}; }
DI void wait_rd4(unsigned fa, unsigned need, unsigned da, f32x4* d) {
    unsigned v, cnt = 1u << 20;
    asm volatile("1:\n\tds_read_b32 %0, %6\n\tds_read_b128 %2, %8\n\tds_read_b128 %3, %8 offset:1024\n\tds_read_b128 %4, %8 offset:2048\n\tds_read_b128 %5, %8 offset:3072\n\t"
                 "s_waitcnt lgkmcnt(0)\n\tv_cmp_gt_u32 vcc, %7, %0\n\ts_cbranch_vccz 2f\n\t"
                 "s_sleep 1\n\ts_sub_u32 %1, %1, 1\n\ts_cbranch_scc0 1b\n2:"
                 : "=&v"(v), "+s"(cnt), "=&v"(d[0]), "=&v"(d[1]), "=&v"(d[2]), "=&v"(d[3]) : "v"(fa), "s"(need), "v"(da) : "memory", "vcc", "scc");
}
DI void wait_rd2(unsigned fa, unsigned need, unsigned da, f32x4* d) {
    unsigned v, cnt = 1u << 20;
    asm volatile("1:\n\tds_read_b32 %0, %4\n\tds_read_b128 %2, %6\n\tds_read_b128 %3, %6 offset:1024\n\t"
                 "s_waitcnt lgkmcnt(0)\n\tv_cmp_gt_u32 vcc, %5, %0\n\ts_cbranch_vccz 2f\n\t"
                 "s_sleep 1\n\ts_sub_u32 %1, %1, 1\n\ts_cbranch_scc0 1b\n2:"
                 : "=&v"(v), "+s"(cnt), "=&v"(d[0]), "=&v"(d[1]) : "v"(fa), "s"(need), "v"(da) : "memory", "vcc", "scc");
}
DI int conv_koff(int w, int tid) { return min(8 * tid, (w == 0 ? 128 : (w == 1 ? 264 : 400)) + (tid << (w == 0 ? 2 : (w == 1 ? 1 : 0)))); }
DI void conv_a(char* cm, unsigned fg, int w, u32x4 kp, int us, u32x4 cur, u32x2 nxt, f32x4* acc) {
    const int lane = threadIdx.x & 63, tid = 64 * w + lane, li = lane & 15, kg = lane >> 4;
    {
        u32x4 o;
        if (w >= 2) {
            const unsigned k00 = pm(kp[0], kp[0], 0x01000100u), k11 = pm(kp[0], kp[0], 0x03020302u);
            o = (u32x4){k11, (tid & 1) ? k11 : pm(kp[0], kp[0], 0x01000302u), k00, k00};
            if (w == 3 && lane < 32) *(u32x4*)(cm + C_KI + lane * 16) = (u32x4){0u, 0u, 0u, 0u};
        } else if (w == 1) {
            o = (u32x4){pm(kp[1], kp[1], 0x01000100u), pm(kp[0], kp[0], 0x03020302u), pm(kp[0], kp[0], 0x01000302u), pm(kp[0], kp[0], 0x01000100u)};
        } else if (tid < 32) {
            o = (u32x4){pm(kp[3], kp[3], 0x01000302u), pm(kp[2], kp[2], 0x01000302u), pm(kp[1], kp[1], 0x01000302u), pm(kp[0], kp[0], 0x01000302u)};
        } else {
            o = (u32x4){pm(kp[1], kp[2], 0x07060100u), pm(kp[1], kp[1], 0x01000100u), pm(kp[0], kp[1], 0x07060100u), pm(kp[0], kp[0], 0x01000100u)};
        }
        *(u32x4*)(cm + C_KI + 512 + tid * 16) = o;
    }
    {
        const unsigned c0 = bf2h(cur[0], us), c1 = bf2h(cur[1], us), c2 = bf2h(cur[2], us), c3 = bf2h(cur[3], us),
                       n0 = bf2h(nxt[0], us), n1 = bf2h(nxt[1], us);
        unsigned s0 = __builtin_amdgcn_alignbit(c1, c0, 16), s1 = __builtin_amdgcn_alignbit(c2, c1, 16),
                 s2 = __builtin_amdgcn_alignbit(c3, c2, 16), s3 = __builtin_amdgcn_alignbit(n0, c3, 16),
                 s4 = __builtin_amdgcn_alignbit(n1, n0, 16);
        char* up = cm + 16 * tid + 32;
        *(u32x4*)(up) = (u32x4){c0, c1, c2, c3};
        *(u32x4*)(up + C_CS) = (u32x4){s0, s1, s2, s3};
        *(u32x4*)(up + 2 * C_CS) = (u32x4){c1, c2, c3, n0};
        *(u32x4*)(up + 3 * C_CS) = (u32x4){s1, s2, s3, s4};
        if (tid < 2) {
            unsigned z0 = tid == 0 ? c0 : 0u, z1 = tid == 0 ? c1 : 0u;
            char* zp = cm + 16 - 16 * tid;
            *(u32x4*)(zp) = (u32x4){0u, 0u, 0u, 0u};
            *(u32x4*)(zp + C_CS) = (u32x4){0u, 0u, 0u, z0 << 16};
            *(u32x4*)(zp + 2 * C_CS) = (u32x4){0u, 0u, 0u, z0};
            *(u32x4*)(zp + 3 * C_CS) = (u32x4){0u, 0u, z0 << 16, __builtin_amdgcn_alignbit(z1, z0, 16)};
        }
    }
    flag_set(fg + 4 * w, 1);
    const unsigned cs = lds_u32(cm);
    unsigned ap = cs + ((li + 1) & 3) * C_CS + 8 * ((li + 1) >> 2) + 16 * kg + 128 * w;
    unsigned bp = cs + C_KI + (33 + 2 * li - kg - 8 * w) * 16;
#pragma unroll
    for (int i = 0; i < 8; ++i) acc[i] = (f32x4){0.f, 0.f, 0.f, 0.f};
    flag_wait(fg + 4 * (lane & 3), 1);
#pragma unroll 1
    for (int rr = 0; rr < 2; ++rr) {
        u32x2 A0[8], A1[8];
        u32x4 B[8];
#pragma unroll
        for (int k = 0; k < 5; ++k) cv_issue(A0[k], A1[k], B[k], ap, bp, k);
#pragma unroll
        for (int k = 0; k < 8; ++k) {
            asm volatile("s_waitcnt lgkmcnt(%3)" : "+v"(A0[k]), "+v"(A1[k]), "+v"(B[k]) : "i"(k < 3 ? 12 : 3 * (7 - k)));
            if (k + 5 < 8) cv_issue(A0[k + 5], A1[k + 5], B[k + 5], ap, bp, k + 5);
#pragma unroll
            for (int j = 0; j <= k; ++j) {
                if (k + j < 8) acc[k + j] = __builtin_amdgcn_mfma_f32_16x16x32_f16(cv_af(A0[k], A1[k]), __builtin_bit_cast(f16x8, B[j]), acc[k + j], 0, 0, 0);
                if (k + j < 8 && j < k) acc[k + j] = __builtin_amdgcn_mfma_f32_16x16x32_f16(cv_af(A0[j], A1[j]), __builtin_bit_cast(f16x8, B[k]), acc[k + j], 0, 0, 0);
            }
        }
        ap += 64; bp -= 64;
    }
    char* red = cm + C_RED + w * 4096 + lane * 16;
    if (w & 2) {
#pragma unroll
        for (int i = 0; i < 4; ++i) *(f32x4*)(red + i * 1024) = acc[i];
        asm volatile("; hi" ::: "memory");
    } else {
#pragma unroll
        for (int i = 0; i < 4; ++i) *(f32x4*)(red + i * 1024) = acc[4 + i];
        asm volatile("; lo" ::: "memory");
    }
    flag_set(fg + 4 * w, 2);
}
DI void conv_b(char* cm, unsigned fg, int w, const f32x4* acc, u32x2 ue0, u32x2 ue1, float ds, int sh, rsrc_t rs, int d) {
    const int lane = threadIdx.x & 63, li = lane & 15, kg = lane >> 4;
    char* red = cm + C_RED + (w ^ 2) * 4096 + lane * 16;
    f32x4 k4[4], s[2];
    wait_rd4(fg + 4 * (w ^ 2), 2, lds_u32(red), k4);
    if (w & 2) {
#pragma unroll
        for (int i = 0; i < 4; ++i) k4[i] = add4(k4[i], acc[4 + i]);
        asm volatile("; hi" : "+v"(k4[0]), "+v"(k4[1]), "+v"(k4[2]), "+v"(k4[3]));
    } else {
#pragma unroll
        for (int i = 0; i < 4; ++i) k4[i] = add4(k4[i], acc[i]);
        asm volatile("; lo" : "+v"(k4[0]), "+v"(k4[1]), "+v"(k4[2]), "+v"(k4[3]));
    }
    if (w & 1) { *(f32x4*)(red) = k4[0]; *(f32x4*)(red + 1024) = k4[1]; asm volatile("; od" ::: "memory"); }
    else { *(f32x4*)(red) = k4[2]; *(f32x4*)(red + 1024) = k4[3]; asm volatile("; ev" ::: "memory"); }
    flag_set(fg + 4 * w, 3);
    float t[8];
#pragma unroll
    for (int e = 0; e < 2; ++e) {
        t[2 * e] = smul(lo2f(ue0[e]), ds); t[2 * e + 1] = smul(hi2f(ue0[e]), ds);
        t[4 + 2 * e] = smul(lo2f(ue1[e]), ds); t[5 + 2 * e] = smul(hi2f(ue1[e]), ds);
    }
    asm volatile("" : "+v"(t[0]), "+v"(t[1]), "+v"(t[2]), "+v"(t[3]), "+v"(t[4]), "+v"(t[5]), "+v"(t[6]), "+v"(t[7]));
    wait_rd2(fg + 4 * (w ^ 1), 3, lds_u32(cm + C_RED + (w ^ 3) * 4096 + lane * 16), s);
    if (w & 1) { s[0] = add4(s[0], k4[2]); s[1] = add4(s[1], k4[3]); asm volatile("; od" : "+v"(s[0]), "+v"(s[1])); }
    else { s[0] = add4(s[0], k4[0]); s[1] = add4(s[1], k4[1]); asm volatile("; ev" : "+v"(s[0]), "+v"(s[1])); }
    u32x2 o[2];
#pragma unroll
    for (int b = 0; b < 2; ++b)
#pragma unroll
        for (int e = 0; e < 2; ++e)
            o[b][e] = pack2(sadd(__builtin_amdgcn_ldexpf(s[b][2 * e], sh), t[4 * b + 2 * e]),
                            sadd(__builtin_amdgcn_ldexpf(s[b][2 * e + 1], sh), t[4 * b + 2 * e + 1]));
    st16(rs, (unsigned)WS_AT + (d * L + 512 * w + 256 * (kg & 1) + 16 * li + 8 * (kg >> 1)) * 2, clean(swap16(o[0], o[1])));
}

struct cf { float r, i; };
DI cf cmul(cf a, cf b) {
    float t0 = a.i * b.i; asm("" : "+v"(t0));
    float t1 = a.i * b.r; asm("" : "+v"(t1));
    cf o;
    o.r = __builtin_fmaf(a.r, b.r, -t0); asm("" : "+v"(o.r));
    o.i = __builtin_fmaf(a.r, b.i, t1); asm("" : "+v"(o.i));
    return o;
}
DI cf cexpP(float a2, float f, float P) {
    float m = __builtin_amdgcn_exp2f(a2 * P);
    float th = __builtin_amdgcn_fractf(f * P);
    cf o;
    o.r = m * __builtin_amdgcn_cosf(th); asm("" : "+v"(o.r));
    o.i = m * __builtin_amdgcn_sinf(th); asm("" : "+v"(o.i));
    return o;
}
DI unsigned pk16(cf v) {
    f32x2 f = {v.r, v.i};
    unsigned h = __builtin_bit_cast(unsigned, __builtin_convertvector(f, f16x2));
    asm volatile("" : "+v"(h));
    return h;
}
template <int ROW> DI void put16(unsigned h) {
    asm volatile("ds_write_addtid_b32 %0 offset:%1" :: "v"(h), "i"(ROW * KG_P) : "memory");
}
template <int N, class F> DI void sfor(F f) { if constexpr (N > 0) { sfor<N - 1>(f); f(std::integral_constant<int, N - 1>{}); } }
DI void slow_wait(unsigned addr, unsigned need) {
    unsigned v, cnt = 1u << 16;
    asm volatile("1:\n\tds_read_b32 %0, %2\n\ts_waitcnt lgkmcnt(0)\n\tv_cmp_gt_u32 vcc, %3, %0\n\ts_cbranch_vccz 2f\n\t"
                 "s_sleep 4\n\ts_sub_u32 %1, %1, 1\n\ts_cbranch_scc0 1b\n2:"
                 : "=&v"(v), "+s"(cnt) : "v"(addr), "v"(need) : "memory", "vcc", "scc");
}

DI void kgen_task(char* scr, unsigned gate, u16 ldt, u16 la, u16 aim, u16 crr, u16 cir) {
    const int lane = threadIdx.x & 63, li = lane & 15, kg = lane >> 4;
    float dt = rbf(expf(bf2f(ldt)));
    float ar = -rbf(expf(bf2f(la)));
    const float a2 = (ar * dt) * 1.44269504088896341f;
    const float f = (bf2f(aim) * dt) * 0.15915494309189535f;
    cf C = {2.f * (bf2f(crr) * dt), 2.f * (bf2f(cir) * dt)};
    unsigned m = max(__float_as_uint(C.r) & 0x7fffffffu, __float_as_uint(C.i) & 0x7fffffffu) >> 23;
    m = max(m, (unsigned)__builtin_amdgcn_update_dpp(0, (int)m, 0x121, 0xf, 0xf, false));
    m = max(m, (unsigned)__builtin_amdgcn_update_dpp(0, (int)m, 0x122, 0xf, 0xf, false));
    m = max(m, (unsigned)__builtin_amdgcn_update_dpp(0, (int)m, 0x124, 0xf, 0xf, false));
    m = max(m, (unsigned)__builtin_amdgcn_update_dpp(0, (int)m, 0x128, 0xf, 0xf, false));
    int e = max(max(__builtin_amdgcn_readlane(m, 0), __builtin_amdgcn_readlane(m, 16)), max(__builtin_amdgcn_readlane(m, 32), __builtin_amdgcn_readlane(m, 48)));
    e = e < 1 ? 1 : (e > 253 ? 253 : e);
    const float sc = __uint_as_float((unsigned)(254 - e) << 23), isc = __uint_as_float((unsigned)e << 23);
    C.r *= sc; C.i *= -sc;
    unsigned hy[16], h0[16], h1[16], h2[9];
    {
        cf w8 = cexpP(a2, -f, 8.f), G[4], Pb[4];
        G[1] = cexpP(a2, -f, 32.f); G[2] = cexpP(a2, -f, 64.f); G[3] = cmul(G[1], G[2]);
        Pb[0] = C; Pb[1] = cmul(C, w8); Pb[2] = cmul(Pb[1], w8); Pb[3] = cmul(Pb[2], w8);
#pragma unroll
        for (int i = 0; i < 16; ++i) hy[i] = pk16(i >> 2 ? cmul(Pb[i & 3], G[i >> 2]) : Pb[i & 3]);
    }
    cf F[8];
    F[0] = {1.f, 0.f};
    F[1] = cexpP(a2, f, 1.f);
    F[2] = cexpP(a2, f, 2.f);
    F[4] = cexpP(a2, f, 4.f);
    F[3] = cmul(F[2], F[1]); F[5] = cmul(F[4], F[1]); F[6] = cmul(F[4], F[2]); F[7] = cmul(F[4], F[3]);
    cf E[8];
    E[1] = cexpP(a2, f, 128.f);
#pragma unroll
    for (int b = 0; b < 8; ++b) { h0[b] = pk16(F[b]); h0[8 + b] = pk16(b ? cmul(E[1], F[b]) : E[1]); }
    E[2] = cexpP(a2, f, 256.f); E[4] = cexpP(a2, f, 512.f);
    E[3] = cmul(E[2], E[1]); E[5] = cmul(E[4], E[1]); E[6] = cmul(E[4], E[2]); E[7] = cmul(E[4], E[3]);
#pragma unroll
    for (int i = 0; i < 8; ++i) h1[i] = pk16(i & 3 ? cmul(E[2 + (i >> 2)], F[2 * (i & 3)]) : E[2 + (i >> 2)]);
#pragma unroll
    for (int a = 0; a < 4; ++a) { h1[8 + 2 * a] = pk16(E[4 + a]); h1[9 + 2 * a] = pk16(cmul(E[4 + a], F[4])); }
    {
        cf w1024 = cexpP(a2, f, 1024.f), w2048 = cexpP(a2, f, 2048.f);
#pragma unroll
        for (int a = 0; a < 8; ++a) h2[a] = pk16(a ? cmul(w1024, E[a]) : w1024);
        h2[8] = pk16(w2048);
    }
    if (gate) slow_wait(gate - 65536 + 4 * (lane & 7), (lane & 4) ? 15u : 24u);
    asm volatile("s_mov_b32 m0, %0\n\ts_nop 0" :: "s"(__builtin_amdgcn_readfirstlane(lds_u32(scr))) : "memory");
    const char* rp = scr + li * KG_P + kg * 16;
    const unsigned ra = lds_u32(rp);
    auto rd4 = [&](u32x4* d) {
        asm volatile("ds_read_b128 %0, %4\n\tds_read_b128 %1, %4 offset:64\n\tds_read_b128 %2, %4 offset:128\n\tds_read_b128 %3, %4 offset:192"
                     : "=&v"(d[0]), "=&v"(d[1]), "=&v"(d[2]), "=&v"(d[3]) : "v"(ra) : "memory");
    };
    u32x4 yh[4], xh[3][4];
    sfor<16>([&](auto I) { put16<I.value>(hy[I.value]); });
    rd4(yh);
    sfor<16>([&](auto I) { put16<I.value>(h0[I.value]); });
    rd4(xh[0]);
    sfor<16>([&](auto I) { put16<I.value>(h1[I.value]); });
    rd4(xh[1]);
    sfor<9>([&](auto I) { put16<I.value>(h2[I.value]); });
    rd4(xh[2]);
    asm volatile("s_waitcnt lgkmcnt(0)" : "+v"(yh[0]), "+v"(yh[1]), "+v"(yh[2]), "+v"(yh[3]), "+v"(xh[0][0]), "+v"(xh[0][1]), "+v"(xh[0][2]), "+v"(xh[0][3]),
                 "+v"(xh[1][0]), "+v"(xh[1][1]), "+v"(xh[1][2]), "+v"(xh[1][3]), "+v"(xh[2][0]), "+v"(xh[2][1]), "+v"(xh[2][2]), "+v"(xh[2][3]) :: "memory");
    f32x4 acc[3];
#pragma unroll
    for (int t = 0; t < 3; ++t) acc[t] = (f32x4){0.f, 0.f, 0.f, 0.f};
#pragma unroll
    for (int s = 0; s < 4; ++s)
#pragma unroll
        for (int t = 0; t < 3; ++t)
            acc[t] = __builtin_amdgcn_mfma_f32_16x16x32_f16(__builtin_bit_cast(f16x8, yh[s]), __builtin_bit_cast(f16x8, xh[t][s]), acc[t], 0, 0, 0);
#pragma unroll
    for (int t = 0; t < 3; ++t) {
        f32x4 o;
#pragma unroll
        for (int r = 0; r < 4; ++r) { float v = acc[t][r] * isc; asm("" : "+v"(v)); o[r] = v; }
        *(f32x4*)(scr + (t * 256 + li * 16 + 4 * kg) * 4) = o;
    }
    *(int*)(scr + 3072) = e;
}

DI void kgen_role(char* grp, char* ktl, unsigned cnt, unsigned gate, int slot, int d0, const u16* __restrict__ logA, const u16* __restrict__ Aim,
                  const u16* __restrict__ Cr, const u16* __restrict__ Ci, const u16* __restrict__ logdt, char* __restrict__ ws) {
    const int lane = threadIdx.x & 63;
    const int c = slot >= 3 ? 1 : 0, th = slot - 3 * c, d = d0 + c, n = d * NS + th * 64 + lane;
    unsigned p0, p1, p2, p3, p4;
    asm volatile("global_load_ushort %0, %5, %6\n\tglobal_load_ushort %1, %7, %8\n\tglobal_load_ushort %2, %7, %9\n\t"
                 "global_load_ushort %3, %7, %10\n\tglobal_load_ushort %4, %7, %11"
                 : "=&v"(p0), "=&v"(p1), "=&v"(p2), "=&v"(p3), "=&v"(p4)
                 : "v"(2 * d), "s"(logdt), "v"(2 * n), "s"(logA), "s"(Aim), "s"(Cr), "s"(Ci) : "memory");
    asm volatile("s_barrier\n\ts_waitcnt vmcnt(0)" : "+v"(p0), "+v"(p1), "+v"(p2), "+v"(p3), "+v"(p4) :: "memory");
    const int e0 = 4 * (th * 56 + lane);
    unsigned ia[2] = {0u, 0u};
#pragma unroll
    for (int i = 0; i < 4; ++i) {
        int e = e0 + i, pp = e;
        if (e >= 256) {
            int oc = (e - 256) / 136, idx = e - 256 - 136 * oc;
            if (oc > 2) { oc = 2; idx = 128; }
            if (idx > 128) idx = 128;
            pp = (128 + idx) << (oc + 1);
        }
        int ex = 24 - __builtin_clz((unsigned)(pp | 128));
        ex = ex > 3 ? 3 : ex;
        int G = 8 * ex + ((((pp >> 7) << 3) | (pp & 7)) >> ex);
        ia[i >> 1] |= (unsigned)(G * 16 + ((pp >> 3) & 15)) << (16 * (i & 1));
    }
    asm volatile("" : "+v"(ia[0]), "+v"(ia[1]));
    kgen_task(grp + slot * KG_S, gate, (u16)p0, (u16)p1, (u16)p2, (u16)p3, (u16)p4);
    {
        const unsigned one = lane == 0 ? 1u : 0u;
        asm volatile("s_waitcnt lgkmcnt(0)\n\tds_add_u32 %0, %1" :: "v"(cnt + 4 * c), "v"(one) : "memory");
    }
    flag_wait(cnt + 4 * c, 3);
    if (lane < 56) {
        const float* t0 = (const float*)(grp + 3 * c * KG_S);
        const int* ep = (const int*)(t0 + 768);
        const int ks = 133 - max(ep[0], max(ep[KG_S / 4], ep[KG_S / 2]));
        _Float16 o[4];
#pragma unroll
        for (int i = 0; i < 4; ++i) {
            const int a = i & 1 ? ia[i >> 1] >> 16 : ia[i >> 1] & 0xffff;
            float v = sadd(sadd(t0[a], t0[a + KG_S / 4]), t0[a + KG_S / 2]);
            o[i] = (_Float16)__builtin_amdgcn_ldexpf(v, ks);
        }
        f16x2 o0 = {o[0], o[1]}, o1 = {o[2], o[3]};
        const u32x2 ov = {__builtin_bit_cast(unsigned, o0), __builtin_bit_cast(unsigned, o1)};
        if (ktl) {
            *(u32x2*)(ktl + c * 1408 + e0 * 2) = ov;
            if (e0 == 0) *(int*)(ktl + c * 1408 + 1344) = ks;
            asm volatile("ds_add_u32 %0, %1" :: "v"(cnt + 4 * c), "v"(lane == 0 ? 1u : 0u) : "memory");
        } else {
            *(u32x2*)(ws + WS_KT + ((size_t)d * KT_N + e0) * 2) = ov;
            if (e0 == 0) *(int*)(ws + WS_KS + d * 4) = ks;
        }
    }
}

DI void ld16(u32x4& v, const char* ws, unsigned off) { asm volatile("global_load_dwordx4 %0, %1, %2 sc1" : "=&v"(v) : "v"(off), "s"(ws) : "memory"); }
DI unsigned mx4(u32x4 v) { return max(max(v[0], v[1]), max(v[2], v[3])); }
DI void gather_issue(u32x4* a, int d0, const char* __restrict__ ws) {
#pragma unroll
    for (int i = 0; i < 8; ++i) ld16(a[i], ws, (unsigned)WS_UT + d0 * L * 2 + i * 1024 + (threadIdx.x & 63) * 16);
}
DI unsigned pmax(unsigned a, unsigned b) { return __builtin_bit_cast(unsigned, __builtin_elementwise_max(__builtin_bit_cast(u16x2, a), __builtin_bit_cast(u16x2, b))); }
DI void transpose_send(const u32x4* tt, int r0, int c0, char* __restrict__ ws);
DI void gather_role(char* smem, u32x4* a, int d0, const u16* __restrict__ x, char* __restrict__ ws, const u32x4* tt, int r0, int c0) {
    const int lane = threadIdx.x & 63;
    auto full = [&]() {
        unsigned m = 0;
#pragma unroll
        for (int i = 0; i < 8; ++i) m = max(m, mx4(a[i]));
        return __builtin_amdgcn_ballot_w64(m == NONE) == 0;
    };
    auto post = [&]() {
        unsigned m[2] = {0u, 0u};
#pragma unroll
        for (int i = 0; i < 8; ++i) {
            *(u32x4*)(smem + F_CV + C_RED + (i >> 2) * C_END + (i & 3) * 1024 + lane * 16) = a[i];
#pragma unroll
            for (int e = 0; e < 4; ++e) m[i >> 2] = pmax(m[i >> 2], a[i][e] & 0x7fff7fffu);
        }
        unsigned mm = pmax(pm(m[1], m[0], 0x05040100u), pm(m[1], m[0], 0x07060302u));
        mm = pmax(mm, (unsigned)__builtin_amdgcn_update_dpp(0, (int)mm, 0x121, 0xf, 0xf, false));
        mm = pmax(mm, (unsigned)__builtin_amdgcn_update_dpp(0, (int)mm, 0x122, 0xf, 0xf, false));
        mm = pmax(mm, (unsigned)__builtin_amdgcn_update_dpp(0, (int)mm, 0x124, 0xf, 0xf, false));
        mm = pmax(mm, (unsigned)__builtin_amdgcn_update_dpp(0, (int)mm, 0x128, 0xf, 0xf, false));
        *(unsigned*)(smem + F_KT + 1360 + (lane >> 4) * 4) = mm;
        flag_set(lds_u32(smem) + 32, 6);
    };
    const bool ok = full();
    if (ok) post();
    transpose_send(tt, r0, c0, ws);
    if (!ok) {
        int n = 0;
#pragma unroll 1
        for (; n < GTRY; ++n) {
            gather_issue(a, d0, ws);
            asm volatile("s_waitcnt vmcnt(0)" : "+v"(a[0]), "+v"(a[1]), "+v"(a[2]), "+v"(a[3]), "+v"(a[4]), "+v"(a[5]), "+v"(a[6]), "+v"(a[7]));
            if (full()) break;
        }
        if (n == GTRY) {
#pragma unroll
            for (int q = 0; q < 4; ++q) {
                unsigned p[8];
#pragma unroll
                for (int i = 0; i < 8; ++i) p[i] = *(const unsigned*)(x + (size_t)(512 * q + 8 * lane + i) * D + d0);
                a[q] = (u32x4){pm(p[1], p[0], 0x05040100u), pm(p[3], p[2], 0x05040100u), pm(p[5], p[4], 0x05040100u), pm(p[7], p[6], 0x05040100u)};
                a[4 + q] = (u32x4){pm(p[1], p[0], 0x07060302u), pm(p[3], p[2], 0x07060302u), pm(p[5], p[4], 0x07060302u), pm(p[7], p[6], 0x07060302u)};
            }
        }
        post();
    }
}

DI void conv1_role(char* smem, int c, int w, int d, const u16* __restrict__ Dskip, char* __restrict__ ws) {
    const int lane = threadIdx.x & 63, tid = 64 * w + lane, li = lane & 15, kg = lane >> 4;
    const unsigned sb = lds_u32(smem);
    const unsigned dk = Dskip[d];
    char* cm = smem + F_CV + c * C_END;
    const char* kt = smem + F_KT + c * 1408;
    flag_wait(lane & 1 ? sb + 32 : sb + 136 + 4 * c, 6);
    const u16* kq = (const u16*)kt + conv_koff(w, tid);
    u32x4 kp = {0u, 0u, 0u, 0u};
    if (w >= 2) kp[0] = kq[0] | ((unsigned)kq[1] << 16);
    else if (w == 1) { kp[0] = ((const unsigned*)kq)[0]; kp[1] = ((const unsigned*)kq)[1]; }
    else { const u32x2 a = ((const u32x2*)kq)[0], b = ((const u32x2*)kq)[1]; kp = (u32x4){a[0], a[1], b[0], b[1]}; }
    const int ks = *(const int*)(kt + 1344);
    const u32x4 mv = *(const u32x4*)(smem + F_KT + 1360);
    const char* ur = cm + C_RED;
    const u32x4 cur = *(const u32x4*)(ur + 16 * tid);
    const u32x2 nxt = *(const u32x2*)(ur + 16 * tid + 16);
    const int t0 = 1024 * w + 32 * li + 8 * kg;
    const u32x2 ue0 = *(const u32x2*)(ur + t0), ue1 = *(const u32x2*)(ur + t0 + 512);
    const unsigned um = pmax(pmax(mv[0], mv[1]), pmax(mv[2], mv[3]));
    const int us = 141 - (int)((c ? um >> 16 : um & 0xffffu) >> 7);
    f32x4 acc[8];
    conv_a(cm, sb + 16 * c, w, kp, us, cur, nxt, acc);
    conv_b(cm, sb + 16 * c, w, acc, ue0, ue1, bf2f((u16)dk), -(us + ks), mk_rsrc(ws), d);
}
static_assert(F_CV % 16 == 0 && F_CV + 2 * C_END <= K1_LDS, "");

DI void transpose_issue(u32x4* tv, int r0, int c0, const u16* __restrict__ x) {
    const int lane = threadIdx.x & 63, ro = lane & 7, cc = lane >> 3;
#pragma unroll
    for (int i = 0; i < 8; ++i)
        asm volatile("global_load_dwordx4 %0, %1, %2" : "=&v"(tv[i]) : "v"(((r0 + 8 * ro + i) * D + c0 + 8 * cc) * 2), "s"(x) : "memory");
}
DI void transpose_send(const u32x4* tt, int r0, int c0, char* __restrict__ ws) {
    const int lane = threadIdx.x & 63, ro = lane & 7, cc = lane >> 3;
    const rsrc_t rs = mk_rsrc(ws);
    const unsigned o = (unsigned)WS_UT + ((c0 + 8 * cc) * L + r0 + 8 * ro) * 2;
#pragma unroll
    for (int j = 0; j < 8; ++j) st16(rs, o + j * L * 2, tt[j]);
}
DI void transpose_regs(const u32x4* tv, u32x4* tt) {
#pragma unroll
    for (int j = 0; j < 4; ++j) {
        tt[2 * j] = clean((u32x4){pm(tv[1][j], tv[0][j], 0x05040100u), pm(tv[3][j], tv[2][j], 0x05040100u),
                                  pm(tv[5][j], tv[4][j], 0x05040100u), pm(tv[7][j], tv[6][j], 0x05040100u)});
        tt[2 * j + 1] = clean((u32x4){pm(tv[1][j], tv[0][j], 0x07060302u), pm(tv[3][j], tv[2][j], 0x07060302u),
                                      pm(tv[5][j], tv[4][j], 0x07060302u), pm(tv[7][j], tv[6][j], 0x07060302u)});
    }
}
DI void transpose_role(int r0, int c0, const u16* __restrict__ x, char* __restrict__ ws) {
    const int lane = threadIdx.x & 63, ro = lane & 7;
    u32x4 tv[8], tt[8];
    transpose_issue(tv, r0, c0, x);
    asm volatile("s_waitcnt vmcnt(0)" : "+v"(tv[0]), "+v"(tv[1]), "+v"(tv[2]), "+v"(tv[3]), "+v"(tv[4]), "+v"(tv[5]), "+v"(tv[6]), "+v"(tv[7]));
    transpose_regs(tv, tt);
    transpose_send(tt, r0, c0, ws);
    unsigned m[4];
#pragma unroll
    for (int j = 0; j < 4; ++j) {
        m[j] = tv[0][j] & 0x7fff7fffu;
#pragma unroll
        for (int i = 1; i < 8; ++i) m[j] = pmax(m[j], tv[i][j] & 0x7fff7fffu);
        m[j] = pmax(m[j], (unsigned)__builtin_amdgcn_update_dpp(0, (int)m[j], 0xb1, 0xf, 0xf, false));
        m[j] = pmax(m[j], (unsigned)__builtin_amdgcn_update_dpp(0, (int)m[j], 0x4e, 0xf, 0xf, false));
        m[j] = pmax(m[j], (unsigned)__builtin_amdgcn_update_dpp(0, (int)m[j], 0x141, 0xf, 0xf, false));
    }
    const unsigned ww = ro & 4 ? (ro & 2 ? m[3] : m[2]) : (ro & 2 ? m[1] : m[0]);
    ((u16*)(ws + WS_UM))[(c0 + lane) * 32 + (r0 >> 6)] = (u16)((ro & 1) ? ww >> 16 : ww);
}

DI void gate_role(int r0, const u16* __restrict__ x, const u16* __restrict__ gw, char* __restrict__ ws) {
    const int lane = threadIdx.x & 63, li = lane & 15, kg = lane >> 4;
    u32x4 tv[10], gwv[5];
#pragma unroll
    for (int j = 0; j < 5; ++j) {
        gwv[j] = *(const u32x4*)(gw + (li + 16 * j) * 8);
#pragma unroll
        for (int rr = 0; rr < 2; ++rr) tv[rr * 5 + j] = *(const u32x4*)(x + (size_t)(r0 + rr * 4 + kg) * D + (li + 16 * j) * 8);
    }
    float* g = (float*)(ws + WS_G);
#pragma unroll
    for (int rr = 0; rr < 2; ++rr) {
        float part = 0.f;
#pragma unroll
        for (int j = 0; j < 5; ++j)
#pragma unroll
            for (int e = 0; e < 4; ++e) {
                unsigned xv = tv[rr * 5 + j][e], wv2 = gwv[j][e];
                part = sadd(part, rbf(smul(lo2f(xv), lo2f(wv2))));
                part = sadd(part, rbf(smul(hi2f(xv), hi2f(wv2))));
            }
        part += __int_as_float(__builtin_amdgcn_update_dpp(0, __float_as_int(part), 0x128, 0xf, 0xf, false));
        part += __int_as_float(__builtin_amdgcn_update_dpp(0, __float_as_int(part), 0x124, 0xf, 0xf, false));
        part += __int_as_float(__builtin_amdgcn_update_dpp(0, __float_as_int(part), 0x122, 0xf, 0xf, false));
        part += __int_as_float(__builtin_amdgcn_update_dpp(0, __float_as_int(part), 0x121, 0xf, 0xf, false));
        if (li == 0) {
            float gs = rbf(part);
            float gg = rbf(1.f / (1.f + expf(-gs)));
            g[r0 + rr * 4 + kg] = gg;
            g[L + r0 + rr * 4 + kg] = rbf(1.f - gg);
        }
    }
}

struct pf_t { const char* ptr[3]; unsigned stp[3]; };
DI void prefetch_init(pf_t& p, int xcd, int fi, const u16* __restrict__ x, const u16* __restrict__ W) {
    const int lane = threadIdx.x & 63;
#pragma unroll
    for (int i = 0; i < 3; ++i) {
        int t = i * 64 + lane;
        if (t >= 184) t -= 64;
        const int r = fi * 184 + t;
        if (r < 512) { p.ptr[i] = (const char*)x + (size_t)(512 * (xcd >> 1) + r) * 1280; p.stp[i] = 128; }
        else {
            int rr = (r - 512) / 15, c = r - 512 - 15 * rr, sg = c / 5;
            p.ptr[i] = (const char*)W + (size_t)rr * 3840 + sg * 1280 + 640 * (xcd & 1) + 128 * (c - 5 * sg);
            p.stp[i] = 64 * 3840;
        }
    }
}
template <int S0, int S1> DI void prefetch_steps(const pf_t& p, unsigned& d) {
#pragma unroll
    for (int st = S0; st < S1; ++st)
#pragma unroll
        for (int i = 0; i < 3; ++i) asm volatile("global_load_dword %0, %1, off" : "+v"(d) : "v"(p.ptr[i] + st * p.stp[i]));
}

DI void wo_load(u32x4* tv, int task, const u16* __restrict__ wo) {
    const int lane = threadIdx.x & 63;
    const int tk = task / 10, tn = task - 10 * tk;
#pragma unroll
    for (int it = 0; it < 8; ++it)
        tv[it] = *(const u32x4*)(wo + (size_t)(64 * tk + it * 8 + (lane >> 3)) * D + 64 * tn + (lane & 7) * 8);
}
DI void wo_role(char* scr, int task, const u32x4* tv, char* __restrict__ ws) {
    const int lane = threadIdx.x & 63, li = lane & 15, kg = lane >> 4;
    const int tk = task / 10, tn = task - 10 * tk;
    const rsrc_t rs = mk_rsrc(ws);
#pragma unroll
    for (int it = 0; it < 8; ++it) *(u32x4*)(scr + (it * 8 + (lane >> 3)) * 144 + (lane & 7) * 16) = tv[it];
    const int q = li >> 2, p = lane & 3;
#pragma unroll
    for (int cb = 0; cb < 4; ++cb)
#pragma unroll
        for (int hh = 0; hh < 2; ++hh) {
            const char* a = scr + (32 * hh + 8 * kg + q) * 144 + cb * 32 + p * 8;
            s16x4 t0 = __builtin_amdgcn_ds_read_tr16_b64_v4i16((LDS s16x4*)(a));
            s16x4 t1 = __builtin_amdgcn_ds_read_tr16_b64_v4i16((LDS s16x4*)(a + 4 * 144));
            bf16x8 v = __builtin_shufflevector(t0, t1, 0, 1, 2, 3, 4, 5, 6, 7);
            st16a(rs, (unsigned)WS_WT + ((2 * tn + (cb >> 1)) * 80 + 8 * tk + 4 * hh + kg) * 512 + ((cb & 1) * 16 + li) * 16, __builtin_bit_cast(u32x4, v));
        }
}

DI void fill_role(char* __restrict__ ws, int bid, int j0) {
    static_assert(2560 + 4 * NC == 256 * 18, "");
    const rsrc_t rs = mk_rsrc(ws);
#pragma unroll 1
    for (int j = j0; j < 18; j += 2) st16(rs, (unsigned)WS_O + (bid * 18 + j) * 1024 + (threadIdx.x & 63) * 16, (u32x4){NONE, NONE, NONE, NONE});
}

extern "C" __global__ __launch_bounds__(1024, 4) void k1(const u16* __restrict__ x, const u16* __restrict__ W,
                                              const u16* __restrict__ logA, const u16* __restrict__ Aim,
                                              const u16* __restrict__ Cr, const u16* __restrict__ Ci,
                                              const u16* __restrict__ logdt, const u16* __restrict__ gw,
                                              char* __restrict__ ws, const u16* __restrict__ wo, const u16* __restrict__ Dskip) {
    extern __shared__ __attribute__((aligned(16))) char smem[];
    const int bid = blockIdx.x;
    const int wave = __builtin_amdgcn_readfirstlane(threadIdx.x >> 6);
    const int xcd = bid & 7, idx = bid >> 3;
    const bool gw_g = idx < 24;
    unsigned pfd = 0;
    if (gw_g && wave == 0) {
        unsigned long long pc;
        unsigned back, fwd;
        asm volatile("s_getpc_b64 %0\n.Lpc_ref:\n\ts_mov_b32 %1, .Lpc_ref-k1\n\ts_mov_b32 %2, .Ltext_end-.Lpc_ref" : "=s"(pc), "=s"(back), "=s"(fwd));
        unsigned off = ((idx & 7) * 64 + threadIdx.x) * 128;
        const unsigned lim = back + fwd - 4;
        off = off < lim ? off : lim;
        asm volatile("global_load_dword %0, %1, %2" : "+v"(pfd) : "v"(off), "s"(pc - back));
    }
    const int rt = 4 * (xcd >> 1) + (idx & 3), ct = idx >> 2;
    const bool isq = ct >= 4;
    const int cg = isq ? 2 * (xcd & 1) + ct - 4 : 4 * (xcd & 1) + ct;
    const char* tab;
    asm volatile("s_getpc_b64 %0\n\ts_branch 1f\n\t.long 1,2,3,4,5,6,7,8,9,10,11,12,13,14,15,16,17,18,19,20,21,22,23,24\n1:" : "=s"(tab));
    if (__builtin_expect(gw_g && (wave >> 2) == 1, 1)) {
        __builtin_amdgcn_s_setprio(PRIO_P);
        producer(smem, x, W, rt, cg, isq, wave - 4, tab, ws);
    }
    if ((gw_g && wave < 4) || (wave & 7) >= 6 && (wave >= 8 || !gw_g)) {
        if (wave == 15 && threadIdx.x < 960 + 36) ((LDS unsigned*)smem)[threadIdx.x - 960] = 0u;
        asm volatile("s_waitcnt lgkmcnt(0)\n\ts_barrier\n\ts_waitcnt vmcnt(0)" : "+v"(pfd) :: "memory");
    }
    if (wave >= 8 || !gw_g) {
        const int w8 = wave & 7, hi = wave >> 3;
        const int rx = 512 * (xcd >> 1);
        const int fd = 128 + 2 * bid;
        if (w8 < 6) {
            if (!hi) __builtin_amdgcn_s_setprio(KPRIO);
            kgen_role(smem + (hi ? K1_KG : G1_BOFF), hi ? nullptr : smem + F_KT, lds_u32(smem) + 128 + (hi ? 0 : 8), gw_g ? lds_u32(smem) + 65536 : 0u, w8,
                      hi ? 2 * bid : fd, logA, Aim, Cr, Ci, logdt, ws);
            if (!gw_g && w8 < 4) conv1_role(smem, hi, w8, fd + hi, Dskip, ws);
        } else if (wave == 6) {
            const int r0 = rx + 64 * (idx & 7), c0 = 512 + 64 * (xcd & 1);
            u32x4 tv[8], tt[8];
            pf_t pf;
            unsigned pd = 0;
            const rsrc_t rs = mk_rsrc(ws);
            __builtin_amdgcn_s_setprio(3);
#pragma unroll
            for (int i = 0; i < 8; ++i) st16(rs, (unsigned)WS_UT + (128 + 2 * bid) * L * 2 + i * 1024 + (threadIdx.x & 63) * 16, (u32x4){NONE, NONE, NONE, NONE});
            transpose_issue(tv, r0, c0, x);
            prefetch_init(pf, xcd, idx - 24, x, W);
            prefetch_steps<PS0, PS1>(pf, pd);
            asm volatile("s_waitcnt vmcnt(%8)" : "+v"(tv[0]), "+v"(tv[1]), "+v"(tv[2]), "+v"(tv[3]), "+v"(tv[4]), "+v"(tv[5]), "+v"(tv[6]), "+v"(tv[7]) : "i"(3 * (PS1 - PS0)));
            transpose_regs(tv, tt);
            transpose_send(tt, r0, c0, ws);
            prefetch_steps<PS1, PS2>(pf, pd);
            gather_issue(tv, 128 + 2 * bid, ws);
            flag_set(lds_u32(smem) + 32, 1);
            asm volatile("s_waitcnt vmcnt(0)" : "+v"(tv[0]), "+v"(tv[1]), "+v"(tv[2]), "+v"(tv[3]), "+v"(tv[4]), "+v"(tv[5]), "+v"(tv[6]), "+v"(tv[7]), "+v"(pd));
            gather_role(smem, tv, 128 + 2 * bid, x, ws, tt, r0, c0);
        }
        if (wave >= 14) {
            slow_wait(lds_u32(smem) + (gw_g ? 4 * (threadIdx.x & 3) : 32), gw_g ? (wave == 14 ? DEFER : DEFER2) : FDEF);
            if (wave == 14) transpose_role(rx + 64 * (idx >> 2), 64 * (4 * (xcd & 1) + (idx & 3)), x, ws);
            else gate_role(rx + 16 * idx + 8 * (xcd & 1), x, gw, ws);
            if (gw_g) slow_wait(lds_u32(smem) + 4 * (threadIdx.x & 3), FILL);
            fill_role(ws, bid, wave - 14);
        }
        if (wave == 7) {
            pf_t pf;
            unsigned pd = 0;
            prefetch_init(pf, xcd, idx - 24, x, W);
            flag_wait(lds_u32(smem) + 32, 1);
            __builtin_amdgcn_s_setprio(WOP);
            prefetch_steps<PS2, 10>(pf, pd);
            const int task = bid - 192, task2 = task < 36 ? task + 64 : task;
            u32x4 tv[8], tw[8];
            wo_load(tv, task, wo);
            wo_load(tw, task2, wo);
            wo_role(smem + F_WO, task, tv, ws);
            if (task < 36) wo_role(smem + F_WO, task2, tw, ws);
            asm volatile("s_waitcnt vmcnt(0)" : "+v"(pd));
        }
    } else if (wave < 4) {
        __builtin_amdgcn_s_setprio(PRIO_C);
        if (isq) gemm_consumer<true>(smem, wave, rt, cg, ws, tab);
        else gemm_consumer<false>(smem, wave, rt, cg, ws, tab);
    }
    asm volatile(".Ltext_k2:");
}

constexpr int M_CV = 256, M_A = M_CV + 2 * C_END, M_KV = M_A + 40960, M_R = M_KV + 4096, M_GA = M_R + 20480, K2_LDS = M_GA + 10240;
static_assert(K2_LDS <= 163840, "");
#define ODLY 3
#define PSL 4
#define ADLY 5
#define GDLY 6
#define BDLY 8
#define P0 2
#define P1 1
#define B0 12
DI u32x4 ld16p(rsrc_t rs, unsigned off) { return __builtin_amdgcn_raw_buffer_load_b128(rs, off, 0, 0); }

__global__ __launch_bounds__(1024, 4) void k2(const u16* __restrict__ Dskip, char* __restrict__ ws, u16* __restrict__ out) {
    extern __shared__ __attribute__((aligned(16))) char smem[];
    const int wave = __builtin_amdgcn_readfirstlane(threadIdx.x >> 6);
    const int lane = threadIdx.x & 63;
    if (wave < 5) *(u32x4*)(smem + (wave < 4 ? M_KV + threadIdx.x * 16 : (lane & 15) * 16)) = (u32x4){0u, 0u, 0u, 0u};
    asm volatile("s_waitcnt lgkmcnt(0)\n\ts_barrier" ::: "memory");
    const int li = lane & 15, kg = lane >> 4, r = lane & 31, h = lane >> 5;
    const int bid = blockIdx.x, xcd = bid & 7, idx = bid >> 3;
    const int row0 = 32 * (8 * xcd + (idx >> 2)), cb = idx & 3;
    const int grp = wave >> 2, w = wave & 3, d = 2 * bid + (grp & 1);
    const bool cv = wave < 8;
    const bool gm = wave < 4 || (wave >= 8 && wave < 14);
    const int gw = wave < 4 ? wave : wave - 4;
    const int kh = gw >= 5 ? 1 : 0, ct = gw - 5 * kh;
    const rsrc_t rs = mk_rsrc(ws);
    const unsigned sb = lds_u32(smem);
    char* cm = smem + M_CV + grp * C_END;
    f32x4 ac[8];
    u32x2 ue0, ue1;
    float ds;
    int sh;
    if (cv) {
        const int tid = threadIdx.x & 255;
        if (grp == 0) __builtin_amdgcn_s_setprio(P0); else __builtin_amdgcn_s_setprio(P1);
        const u32x4 kp = __builtin_amdgcn_raw_buffer_load_b128(rs, conv_koff(w, tid) * 2, (unsigned)WS_KT + d * KT_N * 2, 0);
        unsigned um = __builtin_amdgcn_raw_buffer_load_b32(rs, li * 4, (unsigned)WS_UM + d * 64, 0);
        const unsigned ub = (unsigned)WS_UT + d * L * 2;
        const u32x4 cur = __builtin_amdgcn_raw_buffer_load_b128(rs, 16 * tid, ub, 0);
        const u32x2 nxt = __builtin_amdgcn_raw_buffer_load_b64(rs, 16 * tid + 16, ub, 0);
        const int t0 = 1024 * w + 32 * li + 8 * kg;
        ue0 = __builtin_amdgcn_raw_buffer_load_b64(rs, t0, ub, 0);
        ue1 = __builtin_amdgcn_raw_buffer_load_b64(rs, t0 + 512, ub, 0);
        const int ks = __builtin_amdgcn_raw_buffer_load_b32(rs, 0, (unsigned)WS_KS + d * 4, 0);
        const unsigned dk = Dskip[d];
        flag_set(sb + 80 + 4 * wave, 1);
        um = max(um & 0xffffu, um >> 16);
        um = max(um, (unsigned)__builtin_amdgcn_update_dpp(0, (int)um, 0x121, 0xf, 0xf, false));
        um = max(um, (unsigned)__builtin_amdgcn_update_dpp(0, (int)um, 0x122, 0xf, 0xf, false));
        um = max(um, (unsigned)__builtin_amdgcn_update_dpp(0, (int)um, 0x124, 0xf, 0xf, false));
        um = max(um, (unsigned)__builtin_amdgcn_update_dpp(0, (int)um, 0x128, 0xf, 0xf, false));
        const int us = 141 - (int)(um >> 7);
        conv_a(cm, sb + 16 * grp, w, kp, us, cur, nxt, ac);
        ds = bf2f((u16)dk);
        sh = -(us + ks);
    } else if (wave < 12) {
#pragma unroll 1
        for (int i = 0; i < BDLY; ++i) __builtin_amdgcn_s_sleep(4);
    } else {
        const int hp = xcd + 8 * (idx & 1), orow = 128 * (idx >> 1) + 32 * w + r;
        const int vp = lane + 64 * (w & 1), th = w >> 1;
        f32x4 part[16];
        const unsigned po = (unsigned)WS_KVP + (2 * hp + th) * 2560 + 16 * (vp < 42 ? vp : (vp < 84 ? vp + 22 : (vp < 105 ? vp + 44 : 0)));
        __builtin_amdgcn_s_setprio(3);
#pragma unroll 1
        for (int i = 0; i < ODLY; ++i) __builtin_amdgcn_s_sleep(4);
        flag_wait(sb + 80 + 4 * (lane & 7), 1);
#pragma unroll
        for (int pp = 0; pp < 16; ++pp) part[pp] = __builtin_bit_cast(f32x4, ld16p(rs, po + pp * 81920));
        const unsigned qo = (unsigned)WS_PQ + (5 * hp * L + orow) * 16;
        const u32x4 qa = ld16p(rs, qo + h * L * 16), qb = ld16p(rs, qo + (3 + h) * L * 16), qc = ld16p(rs, h ? 0x7ff00000u : qo + 2 * L * 16);
        f32x4 s = part[0];
#pragma unroll
        for (int pp = 1; pp < 16; ++pp) s = add4(s, part[pp]);
        const int e = vp < 84 ? (vp < 42 ? vp : vp - 42) >> 1 : vp - 84, dq = vp < 84 ? (vp < 42 ? 0 : 2) + (vp & 1) : 4;
        const int m = th == 0 || e == 20 ? e : (e < 4 ? 16 + e : e - 4), sl = th == 0 ? 4 * dq : (dq == 0 ? 20 : 4 * dq - 4);
        if (vp < 105) *(u32x2*)(smem + M_KV + th * 2048 + (sl >> 4) * 1024 + m * 32 + (sl & 15) * 2) = (u32x2){pack2(s[0], s[1]), pack2(s[2], s[3])};
        flag_set(sb + 4 * wave, 1);
        f32x4 kv[4];
        wait_rd4(sb + 48 + 4 * (lane & 3), 1, sb + M_KV + r * 32 + h * 16, kv);
        const unsigned oo = (unsigned)WS_O + (5 * hp * L + orow) * 16;
        u32x4 pt;
#pragma unroll
        for (int hh = 0; hh < 2; ++hh) {
            const u32x4 q1 = hh ? (u32x4){0u, 0u, qc[2], qc[3]} : (u32x4){qc[0], qc[1], 0u, 0u};
            f32x16 n;
#pragma unroll
            for (int i = 0; i < 16; ++i) n[i] = 0.f;
            n = __builtin_amdgcn_mfma_f32_32x32x16_bf16(__builtin_bit_cast(bf16x8, kv[2 * hh]), __builtin_bit_cast(bf16x8, hh ? qb : qa), n, 0, 0, 0);
            n = __builtin_amdgcn_mfma_f32_32x32x16_bf16(__builtin_bit_cast(bf16x8, kv[2 * hh + 1]), __builtin_bit_cast(bf16x8, q1), n, 0, 0, 0);
            const unsigned d0 = __float_as_uint(n[8]);
            float den = __uint_as_float(__builtin_amdgcn_permlane32_swap(d0, d0, false, false)[1]);
            den += 1e-6f;
            float rc = __builtin_amdgcn_rcpf(den);
            rc = __builtin_fmaf(__builtin_fmaf(-den, rc, 1.f), rc, rc);
            float t[12];
#pragma unroll
            for (int i = 0; i < 12; ++i) t[i] = smul(n[i], rc);
            st16(rs, oo + (3 * hh + h) * L * 16, clean(swap32((u32x2){pack2(t[0], t[1]), pack2(t[2], t[3])}, (u32x2){pack2(t[4], t[5]), pack2(t[6], t[7])})));
            pt[2 * hh] = pack2(t[8], t[9]); pt[2 * hh + 1] = pack2(t[10], t[11]);
        }
        if (h == 0) st16(rs, oo + 2 * L * 16, clean(pt));
        if (wave >= 14) {
            const int fh = wave - 14;
#pragma unroll 1
            for (int i = 0; i < ADLY; ++i) __builtin_amdgcn_s_sleep(4);
            const unsigned ao = (unsigned)WS_O + ((40 * fh + h) * L + row0 + r) * 16;
            u32x4 af[20];
#pragma unroll
            for (int i = 0; i < 20; ++i) ld16(af[i], ws, ao + i * 2 * L * 16);
#pragma unroll
            for (int p = 0; p < 5; ++p) {
                u32x4* a = af + 4 * p;
                asm volatile("s_waitcnt vmcnt(%4)" : "+v"(a[0]), "+v"(a[1]), "+v"(a[2]), "+v"(a[3]) : "i"(16 - 4 * p));
#pragma unroll 1
                for (int n = 0; n < (1 << 16); ++n) {
                    if (__builtin_amdgcn_ballot_w64(max(max(mx4(a[0]), mx4(a[1])), max(mx4(a[2]), mx4(a[3]))) == NONE) == 0) break;
                    __builtin_amdgcn_s_sleep(PSL);
#pragma unroll
                    for (int i = 0; i < 4; ++i) ld16(a[i], ws, ao + (4 * p + i) * 2 * L * 16);
                    asm volatile("s_waitcnt vmcnt(0)" : "+v"(a[0]), "+v"(a[1]), "+v"(a[2]), "+v"(a[3]));
                }
#pragma unroll
                for (int i = 0; i < 4; ++i) *(u32x4*)(smem + M_A + (20 * fh + 4 * p + i) * 1024 + lane * 16) = a[i];
                flag_set(sb + 64 + 4 * fh, p + 1);
            }
        }
    }
    u32x4 bf[20];
    const unsigned bo = (unsigned)WS_WT + ((5 * cb + ct) * 40 + 20 * kh) * 1024 + (lane ^ (16 * kh)) * 16;
    asm volatile("" ::: "memory");
    if (gm) {
#pragma unroll
        for (int j = 0; j < B0; ++j) bf[j] = ld16p(rs, bo + j * 1024);
    }
    asm volatile("" ::: "memory");
    if (cv) {
        conv_b(cm, sb + 16 * grp, w, ac, ue0, ue1, ds, sh, rs, d);
    }
    asm volatile("" ::: "memory");
    if (gm) {
        __builtin_amdgcn_s_setprio(0);
#pragma unroll
        for (int j = B0; j < 20; ++j) bf[j] = ld16p(rs, bo + j * 1024);
        const float gg = ((const float*)(ws + WS_G))[row0 + r], omg = ((const float*)(ws + WS_G))[L + row0 + r];
        f32x16 acc;
#pragma unroll
        for (int i = 0; i < 16; ++i) acc[i] = 0.f;
        flag_wait(sb + 64 + 4 * kh, 1);
#pragma unroll
        for (int p = 0; p < 5; ++p) {
            f32x4 a[4];
            wait_rd4(sb + 64 + 4 * kh, p + 1, sb + M_A + (20 * kh + 4 * p) * 1024 + lane * 16, a);
#pragma unroll
            for (int s = 0; s < 4; ++s)
                acc = __builtin_amdgcn_mfma_f32_32x32x16_bf16(__builtin_bit_cast(bf16x8, bf[4 * p + s]), __builtin_bit_cast(bf16x8, a[s]), acc, 0, 0, 0);
        }
        const int pw = kh ? gw - 5 : gw + 5;
        *(f32x4*)(smem + M_R + pw * 2048 + lane * 16) = (f32x4){acc[8], acc[9], acc[10], acc[11]};
        *(f32x4*)(smem + M_R + pw * 2048 + 1024 + lane * 16) = (f32x4){acc[12], acc[13], acc[14], acc[15]};
        flag_set(sb + 128 + 4 * gw, 1);
        f32x4 o[2];
        wait_rd2(sb + 128 + 4 * pw, 1, sb + M_R + gw * 2048 + lane * 16, o);
        float tb[8];
#pragma unroll
        for (int i = 0; i < 8; ++i) tb[i] = smul(omg, sadd(acc[i], o[i >> 2][i & 3]));
        asm volatile("" : "+v"(tb[0]), "+v"(tb[1]), "+v"(tb[2]), "+v"(tb[3]), "+v"(tb[4]), "+v"(tb[5]), "+v"(tb[6]), "+v"(tb[7]));
        u32x2 a[2];
        {
            unsigned v, cnt = 1u << 20;
            asm volatile("1:\n\tds_read_b32 %0, %4\n\tds_read_b64_tr_b16 %2, %5\n\tds_read_b64_tr_b16 %3, %5 offset:512\n\t"
                         "s_waitcnt lgkmcnt(0)\n\tv_cmp_eq_u32 vcc, 0, %0\n\ts_cbranch_vccz 2f\n\t"
                         "s_sleep 1\n\ts_sub_u32 %1, %1, 1\n\ts_cbranch_scc0 1b\n2:"
                         : "=&v"(v), "+s"(cnt), "=&v"(a[0]), "=&v"(a[1])
                         : "v"(sb + 72 + 4 * kh), "v"(sb + M_GA + gw * 1024 + (4 * h + (li >> 2)) * 64 + (kg & 1) * 32 + (lane & 3) * 8) : "memory", "vcc", "scc");
        }
        u32x2 v[2];
#pragma unroll
        for (int g = 0; g < 2; ++g) {
            float t[4];
#pragma unroll
            for (int e = 0; e < 4; ++e) t[e] = sfma(gg, e & 1 ? hi2f(a[g][e >> 1]) : lo2f(a[g][e >> 1]), tb[4 * g + e]);
            v[g] = (u32x2){pack2(t[0], t[1]), pack2(t[2], t[3])};
        }
        *(u32x4*)(out + (size_t)(row0 + r) * D + 160 * cb + 32 * ct + 16 * kh + 8 * h) = swap32(v[0], v[1]);
    } else if (wave < 6) {
        const int fh = wave - 4;
        __builtin_amdgcn_s_setprio(3);
#pragma unroll 1
        for (int i = 0; i < GDLY; ++i) __builtin_amdgcn_s_sleep(4);
        const unsigned go = (unsigned)WS_AT + ((160 * cb + 16 * fh + (lane >> 2)) * L + row0 + 8 * (lane & 3)) * 2;
        u32x4 af[5];
#pragma unroll 1
        for (int n = 0; n < (1 << 16); ++n) {
#pragma unroll
            for (int i = 0; i < 5; ++i) ld16(af[i], ws, go + i * 32 * L * 2);
            asm volatile("s_waitcnt vmcnt(0)" : "+v"(af[0]), "+v"(af[1]), "+v"(af[2]), "+v"(af[3]), "+v"(af[4]));
            unsigned m = 0;
#pragma unroll
            for (int i = 0; i < 5; ++i) m = max(m, mx4(af[i]));
            if (__builtin_amdgcn_ballot_w64(m == NONE) == 0) break;
            __builtin_amdgcn_s_sleep(PSL);
        }
#pragma unroll
        for (int i = 0; i < 5; ++i) *(u32x4*)(smem + M_GA + (5 * fh + i) * 1024 + lane * 16) = af[i];
        flag_set(sb + 72 + 4 * fh, 1);
    }
    asm volatile(".Ltext_end:");
}

extern "C" void kernel_launch(void* const* d_in, const int* in_sizes, int n_in, void* d_out, int out_size,
                              void* d_ws, size_t ws_size, hipStream_t stream) {
    const u16* u = (const u16*)d_in[0];
    const u16* logA = (const u16*)d_in[1];
    const u16* Aim = (const u16*)d_in[2];
    const u16* Cr = (const u16*)d_in[3];
    const u16* Ci = (const u16*)d_in[4];
    const u16* logdt = (const u16*)d_in[5];
    const u16* Dskip = (const u16*)d_in[6];
    const u16* wqkv = (const u16*)d_in[8];
    const u16* wo = (const u16*)d_in[9];
    const u16* gw = (const u16*)d_in[10];
    char* ws = (char*)d_ws;
    (void)hipFuncSetAttribute((const void*)k1, hipFuncAttributeMaxDynamicSharedMemorySize, K1_LDS);
    (void)hipFuncSetAttribute((const void*)k2, hipFuncAttributeMaxDynamicSharedMemorySize, K2_LDS);
    k1<<<256, 1024, K1_LDS, stream>>>(u, wqkv, logA, Aim, Cr, Ci, logdt, gw, ws, wo, Dskip);
    k2<<<256, 1024, K2_LDS, stream>>>(Dskip, ws, (u16*)d_out);
}
```
